```python
import torch

def _softmax(x, axis=-1):
    return torch.softmax(x, dim=axis)
def _gelu(x):
    return torch.nn.functional.gelu(x, approximate="tanh")
def _silu(x):
    return torch.nn.functional.silu(x)
def _sigmoid(x):
    return torch.sigmoid(x)
def _layernorm(x, axis=-1, eps=1e-5):
    m = x.mean(dim=axis, keepdim=True)
    v = x.var(dim=axis, keepdim=True, unbiased=False)
    return (x - m) / torch.sqrt(v + eps)
def _scatter_add(tgt, idx, src, axis=0):
    return tgt.clone().index_add_(axis, idx, src)
def _segment_sum(src, seg, n):
    out = torch.zeros((n,) + src.shape[1:], dtype=src.dtype)
    return out.index_add_(0, seg, src)
def _pad(x, pw):
    flat = tuple(v for pair in reversed(pw) for v in pair)
    return torch.nn.functional.pad(x, flat)

kernel_name = 'sequential_preLN__mha_block__dense_activation_relu__prompt'

def setup_inputs():

    _w = (lambda a: (lambda t: t.to(torch.bfloat16) if t.is_floating_point() else t)(torch.as_tensor(a)))
    def _A_setup():
        B, S, D, H = 1, 2048, 512, 32
        return {
            "x": _w((lambda *s: torch.randn(*s, dtype=torch.bfloat16) * 0.05)(B, S, D)),
            "wqkv": _w((lambda *s: torch.randn(*s, dtype=torch.bfloat16) * 0.05)(D, 3 * D)),
            "wo": _w((lambda *s: torch.randn(*s, dtype=torch.bfloat16) * 0.05)(D, D)),
        }
    _A_in = _A_setup()

    def _B_setup():
        B, S, D, Dff = 1, 2048, 512, 1792
        return {
            "x": _w((lambda *s: torch.randn(*s, dtype=torch.bfloat16) * 0.05)(B, S, D)),
            "w1": _w((lambda *s: torch.randn(*s, dtype=torch.bfloat16) * 0.05)(D, Dff)),
            "w2": _w((lambda *s: torch.randn(*s, dtype=torch.bfloat16) * 0.05)(Dff, D)),
        }
    _B_in = _B_setup()

    return {
        "A_x": _A_in["x"],
        "A_wqkv": _A_in["wqkv"],
        "A_wo": _A_in["wo"],
        "B_x": _B_in["x"],
        "B_w1": _B_in["w1"],
        "B_w2": _B_in["w2"],
    }

def reference(A_x, A_wqkv, A_wo, B_x, B_w1, B_w2):
    def _A_ref(x, wqkv, wo):
        B, S, D = x.shape
        H = 32
        Dh = D // H
        qkv = (lambda x, *d: x.permute(*d))((x @ wqkv).reshape(B, S, 3, H, Dh), 2, 0, 3, 1, 4)
        q, k, v = qkv[0], qkv[1], qkv[2]
        scores = torch.einsum("bhqd,bhkd->bhqk", q, k) * (Dh ** -0.5)
        p = _softmax(scores, axis=-1)
        o = torch.einsum("bhqk,bhkd->bhqd", p, v)
        return (lambda x, *d: x.permute(*d))(o, 0, 2, 1, 3).reshape(B, S, D) @ wo

    def _B_ref(x, w1, w2):
        h = torch.einsum("bsd,df->bsf", x, w1)
        zero = torch.zeros_like(h)
        h = torch.maximum(h, zero)
        return torch.einsum("bsf,fd->bsd", h, w2)

    x = A_x
    h = x + _A_ref(_layernorm(x, axis=-1), A_wqkv, A_wo)
    h2 = h + _B_ref(_layernorm(h, axis=-1), B_w1, B_w2)
    return h2
```

```cpp
#include <hip/hip_runtime.h>
#include <hip/hip_bf16.h>
#include <stdint.h>

namespace blk {
typedef float f32x16 __attribute__((ext_vector_type(16)));
typedef float f32x8 __attribute__((ext_vector_type(8)));
typedef float f32x4 __attribute__((ext_vector_type(4)));
typedef unsigned u32x4 __attribute__((ext_vector_type(4)));
typedef unsigned u32x2 __attribute__((ext_vector_type(2)));
typedef __bf16 bf16x8 __attribute__((ext_vector_type(8)));
typedef __bf16 bf16x16 __attribute__((ext_vector_type(16)));
typedef __bf16 bf16x2 __attribute__((ext_vector_type(2)));
typedef short s16x4 __attribute__((ext_vector_type(4)));
#define LAS __attribute__((address_space(3)))
#define DEV __device__ __forceinline__

constexpr int S = 2048, D = 512, H = 32, DFF = 1792;
constexpr size_t MB = 1u << 20;
constexpr size_t QF_OFF = 0, KV_OFF = 2 * MB, OF_OFF = 6 * MB, HF_OFF = 8 * MB, H1_OFF = 10 * MB,
                 WO_OFF = 18 * MB, W1_OFF = WO_OFF + MB / 2, W2_OFF = W1_OFF + 7 * MB / 4, KM_OFF = 22 * MB;
constexpr float QSCALE = 0.25f * 1.4426950408889634f;
constexpr float LN_EPS = 1e-5f;
constexpr unsigned ONES2 = 0x3F803F80u;

DEV f32x16 mfma(u32x4 a, u32x4 b, f32x16 c) {
  return __builtin_amdgcn_mfma_f32_32x32x16_bf16(__builtin_bit_cast(bf16x8, a), __builtin_bit_cast(bf16x8, b), c, 0, 0, 0);
}
DEV float dot2(unsigned a, unsigned b, float c) {
  return __builtin_amdgcn_fdot2_f32_bf16(__builtin_bit_cast(bf16x2, a), __builtin_bit_cast(bf16x2, b), c, false);
}
DEV unsigned pk(float lo, float hi) {
  bf16x2 r = {(__bf16)lo, (__bf16)hi};
  return __builtin_bit_cast(unsigned, r);
}
DEV float lo16(unsigned u) { return __uint_as_float(u << 16); }
DEV float hi16(unsigned u) { return __uint_as_float(u & 0xffff0000u); }
DEV float halfsum(float v) {
  auto r = __builtin_amdgcn_permlane32_swap(__float_as_uint(v), __float_as_uint(v), false, false);
  return __uint_as_float(r[0]) + __uint_as_float(r[1]);
}
DEV float halfmax(float v) {
  auto r = __builtin_amdgcn_permlane32_swap(__float_as_uint(v), __float_as_uint(v), false, false);
  return fmaxf(__uint_as_float(r[0]), __uint_as_float(r[1]));
}
DEV void glds(const void* base, unsigned voff, unsigned lds) {
  asm volatile("s_mov_b32 m0, %2\n\ts_nop 0\n\tglobal_load_lds_dwordx4 %0, %1" ::"v"(voff), "s"(base), "s"(lds) : "memory");
}
DEV u32x4 ldsr128(unsigned addr) { return *(const LAS u32x4*)addr; }
DEV f32x4 ldsr128f(unsigned addr) { return *(const LAS f32x4*)addr; }
DEV u32x2 ldstr(unsigned addr) {
  return __builtin_bit_cast(u32x2, __builtin_amdgcn_ds_read_tr16_b64_v4i16((LAS s16x4*)addr));
}
#define WAITV(n) asm volatile("s_waitcnt vmcnt(" #n ")" ::: "memory")
#define BARRIER() asm volatile("s_waitcnt lgkmcnt(0)\n\ts_barrier" ::: "memory")
#define RFL(x) __builtin_amdgcn_readfirstlane(x)

constexpr int K1_RING = 4 * 32768, K1_PT = K1_RING, K1_ST = K1_PT + 16384, K1_CS = K1_ST + 512, K1_LDS = K1_CS + 4 * 256;
__global__ __launch_bounds__(512) void k_qkv(const char* __restrict__ x, const char* __restrict__ wqkv, const char* __restrict__ wo,
                                             const char* __restrict__ w1, const char* __restrict__ w2, char* __restrict__ ws) {
  extern __shared__ __attribute__((aligned(16))) char smem[];
  const unsigned lds0 = (unsigned)(uintptr_t)smem;
  const int tid = threadIdx.x, lane = tid & 63, w = RFL(tid >> 6), hh = lane >> 5;
  const int g = blockIdx.x & 7, r = blockIdx.x >> 3;
  const int rb = (g >> 1) * 8 + (r & 7), hq = (g & 1) * 4 + (r >> 3);
  const int srow = 8 * w + (lane >> 3), sc = lane & 7;
  const unsigned vx = (unsigned)(srow * 1024 + ((sc ^ ((srow >> 1) & 7)) << 4));
  const unsigned vw = (unsigned)(srow * 3072 + ((sc ^ (((srow >> 1) & 1) << 2)) << 4));
  const char* xb = x + (size_t)rb * 65536;
  const char* wb = wqkv + hq * 128;
  auto stage = [&](int c) {
    const unsigned l = lds0 + (c & 3) * 32768 + w * 1024;
    glds(xb + c * 128, vx, l);
    glds(wb + (size_t)c * 196608, vw, l + 8192);
    glds(wb + (size_t)c * 196608 + 1024, vw, l + 16384);
    glds(wb + (size_t)c * 196608 + 2048, vw, l + 24576);
  };
  stage(0); stage(1); stage(2); stage(3);
  if (w < 4) {
    const int tt = w & 1, hp = w >> 1;
    const int xr = 32 * tt + (lane & 31);
    const unsigned ax = lds0 + xr * 128 + ((hh ^ ((xr >> 1) & 7)) << 4);
    const int q = (lane & 15) >> 2, p = lane & 3, blk = (lane >> 4) & 1;
    const unsigned aw = lds0 + 8192 + (8 * hh + q) * 128 + (((4 * hp + 2 * blk + (p >> 1)) ^ (((q >> 1) & 1) << 2)) << 4) + 8 * (p & 1);
    f32x16 aq = {}, ak = {}, av = {};
    float cq = 0.f, ck = 0.f, cv = 0.f;
#pragma unroll
    for (int c = 0; c < 8; ++c) {
      if (c == 0) WAITV(12); else if (c < 6) WAITV(8); else if (c == 6) WAITV(4); else WAITV(0);
      BARRIER();
      if (c >= 1 && c + 3 < 8) stage(c + 3);
      const unsigned so = (c & 3) * 32768;
#pragma unroll
      for (int s = 0; s < 4; ++s) {
        const u32x4 xf = ldsr128((ax ^ (s << 5)) + so);
        u32x4 wf[3];
#pragma unroll
        for (int j = 0; j < 3; ++j) {
          const u32x2 a = ldstr(aw + so + j * 8192 + s * 2048), b = ldstr(aw + so + j * 8192 + s * 2048 + 512);
          wf[j] = u32x4{a[0], a[1], b[0], b[1]};
        }
        aq = mfma(wf[0], xf, aq);
        ak = mfma(wf[1], xf, ak);
        av = mfma(xf, wf[2], av);
#pragma unroll
        for (int j = 0; j < 4; ++j) { cq = dot2(wf[0][j], ONES2, cq); ck = dot2(wf[1][j], ONES2, ck); cv = dot2(wf[2][j], ONES2, cv); }
      }
    }
    cq = halfsum(cq); ck = halfsum(ck); cv = halfsum(cv);
    const unsigned csb = lds0 + K1_CS + w * 256;
    *(LAS float*)(csb + hh * 128 + (lane & 31) * 4) = hh ? ck : cq;
    BARRIER();
    const float rstd = *(const LAS float*)(lds0 + K1_ST + xr * 4), nmr = *(const LAS float*)(lds0 + K1_ST + 256 + xr * 4);
    const int ttile = rb * 2 + tt, h0 = hq * 4 + hp * 2;
    {
      const float rq = rstd * QSCALE, nq = nmr * QSCALE;
      float vq[16], vk[16];
#pragma unroll
      for (int i = 0; i < 4; ++i) {
        const f32x4 c1 = ldsr128f(csb + (8 * i + 4 * hh) * 4), c2 = ldsr128f(csb + 128 + (8 * i + 4 * hh) * 4);
#pragma unroll
        for (int j = 0; j < 4; ++j) { vq[4 * i + j] = aq[4 * i + j] * rq + nq * c1[j]; vk[4 * i + j] = ak[4 * i + j] * rstd + nmr * c2[j]; }
      }
      const int t = lane & 31, tp = (t & 19) | ((t & 4) << 1) | ((t & 8) >> 1);
#pragma unroll
      for (int s = 0; s < 2; ++s) {
        const u32x4 fq = {pk(vq[8 * s], vq[8 * s + 1]), pk(vq[8 * s + 2], vq[8 * s + 3]), pk(vq[8 * s + 4], vq[8 * s + 5]), pk(vq[8 * s + 6], vq[8 * s + 7])};
        const u32x4 fk = {pk(vk[8 * s], vk[8 * s + 1]), pk(vk[8 * s + 2], vk[8 * s + 3]), pk(vk[8 * s + 4], vk[8 * s + 5]), pk(vk[8 * s + 6], vk[8 * s + 7])};
        *(u32x4*)(ws + QF_OFF + ((size_t)(h0 + s) * 64 + ttile) * 1024 + lane * 16) = fq;
        *(u32x4*)(ws + KV_OFF + ((size_t)(h0 + s) * 64 + ttile) * 2048 + (hh * 32 + tp) * 16) = fk;
        float n2 = 0.f;
#pragma unroll
        for (int j = 0; j < 4; ++j) n2 = dot2(fk[j], fk[j], n2);
        n2 = halfsum(n2);
#pragma unroll
        for (int o = 1; o < 32; o <<= 1) { const float y = __shfl_xor(n2, o); n2 = (y > n2 || y != y) ? y : n2; }
        if (lane == 0) *(float*)(ws + KM_OFF + ((h0 + s) * 64 + ttile) * 4) = n2;
      }
    }
    {
      float vv[16];
#pragma unroll
      for (int i = 0; i < 4; ++i) {
        const f32x4 rs = ldsr128f(lds0 + K1_ST + (32 * tt + 8 * i + 4 * hh) * 4), nm = ldsr128f(lds0 + K1_ST + 256 + (32 * tt + 8 * i + 4 * hh) * 4);
#pragma unroll
        for (int j = 0; j < 4; ++j) vv[4 * i + j] = av[4 * i + j] * rs[j] + nm[j] * cv;
      }
      char* vb = ws + KV_OFF + ((size_t)(h0 + blk) * 64 + ttile) * 2048 + 1024 + hh * 256 + (lane & 15) * 16;
#pragma unroll
      for (int s = 0; s < 2; ++s)
        *(u32x4*)(vb + s * 512) = u32x4{pk(vv[8 * s], vv[8 * s + 1]), pk(vv[8 * s + 2], vv[8 * s + 3]), pk(vv[8 * s + 4], vv[8 * s + 5]), pk(vv[8 * s + 6], vv[8 * s + 7])};
    }
  } else {
    const int hw = w - 4;
    const int job = blockIdx.x * 4 + hw;
    const char* src; char* dst; int pitch, nks, kb, nb;
    if (job < 128) { src = wo; dst = ws + WO_OFF; pitch = 1024; nks = 32; kb = job >> 3; nb = job & 7; }
    else if (job < 576) { const int j = job - 128; src = w1; dst = ws + W1_OFF; pitch = 3584; nks = 32; kb = j / 28; nb = j % 28; }
    else { const int j = job - 576; src = w2; dst = ws + W2_OFF; pitch = 1024; nks = 112; kb = j >> 3; nb = j & 7; }
    const unsigned ptl = lds0 + K1_PT + hw * 4096;
    {
      const int pr = lane >> 3;
      const unsigned vp = (unsigned)(pr * pitch + ((sc ^ (((pr >> 1) & 1) << 2)) << 4));
      const char* sb = src + (size_t)kb * 32 * pitch + nb * 128;
#pragma unroll
      for (int i = 0; i < 4; ++i) glds(sb + (size_t)i * 8 * pitch, vp, ptl + i * 1024);
    }
    float s0 = 0.f, s1 = 0.f, q0 = 0.f, q1 = 0.f;
    const unsigned ah = lds0 + hw * 2048 + lane * 16;
#pragma unroll
    for (int c = 0; c < 8; ++c) {
      if (c == 0) WAITV(16); else if (c < 4) WAITV(12); else if (c == 4) WAITV(8); else if (c == 5) WAITV(12); else if (c == 6) WAITV(8); else WAITV(4);
      BARRIER();
      if (c >= 1 && c + 3 < 8) stage(c + 3);
      const u32x4 a = ldsr128(ah + (c & 3) * 32768), b = ldsr128(ah + (c & 3) * 32768 + 1024);
#pragma unroll
      for (int j = 0; j < 4; ++j) { s0 = dot2(a[j], ONES2, s0); q0 = dot2(a[j], a[j], q0); s1 = dot2(b[j], ONES2, s1); q1 = dot2(b[j], b[j], q1); }
      if (c == 4) {
        const int q = (lane & 15) >> 2, p = lane & 3, blk = (lane >> 4) & 1;
#pragma unroll
        for (int n = 0; n < 2; ++n) {
          const unsigned ap = ptl + (4 * hh + q) * 128 + (((4 * n + 2 * blk + (p >> 1)) ^ (((q >> 1) & 1) << 2)) << 4) + 8 * (p & 1);
#pragma unroll
          for (int s = 0; s < 2; ++s) {
            const u32x2 u = ldstr(ap + s * 2048), v = ldstr(ap + s * 2048 + 1024);
            *(u32x4*)(dst + ((size_t)(nb * 2 + n) * nks + kb * 2 + s) * 1024 + lane * 16) = u32x4{u[0], u[1], v[0], v[1]};
          }
        }
        asm volatile("" ::: "memory");
      }
    }
#pragma unroll
    for (int o = 1; o < 8; o <<= 1) { s0 += __shfl_xor(s0, o); s1 += __shfl_xor(s1, o); q0 += __shfl_xor(q0, o); q1 += __shfl_xor(q1, o); }
    const float sm = (lane & 1) ? s1 : s0, sq = (lane & 1) ? q1 : q0;
    const float mu = sm * (1.f / D), var = fmaxf(sq * (1.f / D) - mu * mu, 0.f), rstd = rsqrtf(var + LN_EPS);
    if ((lane & 7) < 2) {
      const int row = hw * 16 + (lane & 1) * 8 + (lane >> 3);
      *(LAS float*)(lds0 + K1_ST + row * 4) = rstd;
      *(LAS float*)(lds0 + K1_ST + 256 + row * 4) = -mu * rstd;
    }
    BARRIER();
  }
}

constexpr int TS = 2064;
constexpr int K2_LDS = 64 * TS;
constexpr float SAFE_BOUND = 48.f;
DEV f32x16 smf(u32x4 a, f32x8 b, f32x16 c) {
  return __builtin_amdgcn_smfmac_f32_32x32x32_bf16(__builtin_bit_cast(bf16x8, a), __builtin_bit_cast(bf16x16, b), c, 0xDDDD, 0, 0);
}
#define SB() __builtin_amdgcn_sched_barrier(0)
__global__ __launch_bounds__(512) void k_attn(char* __restrict__ ws) {
  extern __shared__ __attribute__((aligned(16))) char smem[];
  const unsigned lds0 = (unsigned)(uintptr_t)smem;
  const int tid = threadIdx.x, lane = tid & 63, w = RFL(tid >> 6), hh = lane >> 5;
  const int g = blockIdx.x & 7, r = blockIdx.x >> 3;
  const int h = 4 * g + (r >> 3), qt = (r & 7) * 8 + w;
  const u32x4 qf = *(const u32x4*)(ws + QF_OFF + ((size_t)h * 64 + qt) * 1024 + lane * 16);
  float km = *(const float*)(ws + KM_OFF + (h * 64 + lane) * 4);
  {
    const char* kvb = ws + KV_OFF + (size_t)h * 131072 + w * 1024;
    const unsigned l = lds0 + (w >> 1) * TS + (w & 1) * 1024;
#pragma unroll
    for (int i = 0; i < 16; ++i) glds(kvb + i * 8192, lane * 16, l + i * 4 * TS);
  }
  if (w == 0) *(LAS u32x4*)(lds0 + lane * TS + 2048) = u32x4{ONES2, ONES2, ONES2, ONES2};
#pragma unroll
  for (int o = 1; o < 64; o <<= 1) { const float y = __shfl_xor(km, o); km = (y > km || y != y) ? y : km; }
  float q2 = 0.f;
#pragma unroll
  for (int j = 0; j < 4; ++j) q2 = dot2(qf[j], qf[j], q2);
  float m = sqrtf(halfsum(q2) * km);
  const int rr = lane & 31;
  const unsigned aK = lds0 + lane * 16;
  const bool isones = (rr >= 16 && rr < 20) || rr >= 28;
  const unsigned aV0 = lds0 + (isones ? 2048 : 1024 + hh * 256 + (rr < 16 ? rr * 16 : 0));
  const unsigned aV1 = aV0 + (isones ? 0 : 512);
  WAITV(0);
  BARRIER();
  if (!__all(m <= SAFE_BOUND)) {
    float mx = -INFINITY;
    for (int t = 0; t < 64; ++t) {
      const f32x16 s = mfma(ldsr128(aK + t * TS), qf, f32x16{});
#pragma unroll
      for (int i = 0; i < 16; ++i) mx = fmaxf(mx, s[i]);
    }
    m = halfmax(mx);
  }
  f32x16 nm;
#pragma unroll
  for (int i = 0; i < 16; ++i) nm[i] = -m;
  asm volatile("" : "+v"(nm));
  f32x16 o = {};
  f32x16 sa = mfma(ldsr128(aK), qf, nm), sb;
#define LO(x) __builtin_shufflevector(x, x, 0, 1, 2, 3, 4, 5, 6, 7)
#define HI(x) __builtin_shufflevector(x, x, 8, 9, 10, 11, 12, 13, 14, 15)
#define EXPN(x, a, b) _Pragma("unroll") for (int i_ = a; i_ < b; ++i_) x[i_] = __builtin_amdgcn_exp2f(x[i_])
  sb = mfma(ldsr128(aK + TS), qf, nm);
  EXPN(sa, 0, 16);
  for (int t = 0; t < 62; t += 2) {
    const unsigned o0 = t * TS;
    {
      const u32x4 v0 = ldsr128(aV0 + o0), v1 = ldsr128(aV1 + o0), k2 = ldsr128(aK + o0 + 2 * TS);
      EXPN(sb, 0, 4);
      o = smf(v0, LO(sa), o);
      EXPN(sb, 4, 7);
      o = smf(v1, HI(sa), o);
      EXPN(sb, 7, 10);
      sa = mfma(k2, qf, nm);
      EXPN(sb, 10, 16);
    }
    {
      const u32x4 v0 = ldsr128(aV0 + o0 + TS), v1 = ldsr128(aV1 + o0 + TS), k2 = ldsr128(aK + o0 + 3 * TS);
      EXPN(sa, 0, 4);
      o = smf(v0, LO(sb), o);
      EXPN(sa, 4, 7);
      o = smf(v1, HI(sb), o);
      EXPN(sa, 7, 10);
      sb = mfma(k2, qf, nm);
      EXPN(sa, 10, 16);
    }
  }
  o = smf(ldsr128(aV0 + 62 * TS), LO(sa), o);
  o = smf(ldsr128(aV1 + 62 * TS), HI(sa), o);
  EXPN(sb, 0, 16);
  o = smf(ldsr128(aV0 + 63 * TS), LO(sb), o);
  o = smf(ldsr128(aV1 + 63 * TS), HI(sb), o);
  const float inv = 1.f / (hh ? o[12] : o[8]);
  *(u32x4*)(ws + OF_OFF + ((size_t)qt * 32 + h) * 1024 + lane * 16) =
      u32x4{pk(o[0] * inv, o[1] * inv), pk(o[2] * inv, o[3] * inv), pk(o[4] * inv, o[5] * inv), pk(o[6] * inv, o[7] * inv)};
}

constexpr int K3_LDS = 8 * 4 * 2048;
__global__ __launch_bounds__(512) void k_oproj(const char* __restrict__ x, char* __restrict__ ws) {
  extern __shared__ __attribute__((aligned(16))) char smem[];
  const unsigned lds0 = (unsigned)(uintptr_t)smem;
  const int tid = threadIdx.x, lane = tid & 63, w = RFL(tid >> 6), hh = lane >> 5;
  const int g = blockIdx.x & 7, r = blockIdx.x >> 3, tb = 4 * g + (r >> 3), c = r & 7;
  const int tt = w >> 2, kq = w & 3;
  u32x4 wf[2][8], af[8];
#pragma unroll
  for (int i = 0; i < 8; ++i) af[i] = *(const u32x4*)(ws + OF_OFF + ((size_t)(2 * tb + tt) * 32 + 8 * kq + i) * 1024 + lane * 16);
#pragma unroll
  for (int i = 0; i < 8; ++i)
#pragma unroll
    for (int n = 0; n < 2; ++n) wf[n][i] = *(const u32x4*)(ws + WO_OFF + ((size_t)(2 * c + n) * 32 + 8 * kq + i) * 1024 + lane * 16);
  const char* xp = x + (size_t)(64 * tb + 32 * tt + (lane & 31)) * 1024 + (64 * c + 16 * kq + 4 * hh) * 2;
  const u32x2 x0 = *(const u32x2*)xp, x1 = *(const u32x2*)(xp + 16);
  f32x16 acc[2] = {};
#pragma unroll
  for (int i = 0; i < 8; ++i) { acc[0] = mfma(wf[0][i], af[i], acc[0]); acc[1] = mfma(wf[1][i], af[i], acc[1]); }
  float own[8];
#pragma unroll
  for (int j = 0; j < 4; ++j) {
    const f32x16& a = acc[j >> 1];
    const int b = 8 * (j & 1);
    if (j == kq) {
#pragma unroll
      for (int i = 0; i < 8; ++i) own[i] = a[b + i];
    } else {
      const unsigned d = lds0 + ((tt * 4 + j) * 4 + kq) * 2048 + lane * 16;
      *(LAS f32x4*)d = f32x4{a[b], a[b + 1], a[b + 2], a[b + 3]};
      *(LAS f32x4*)(d + 1024) = f32x4{a[b + 4], a[b + 5], a[b + 6], a[b + 7]};
    }
  }
  BARRIER();
#pragma unroll
  for (int j = 1; j < 4; ++j) {
    const unsigned d = lds0 + (w * 4 + (kq ^ j)) * 2048 + lane * 16;
    const f32x4 u = ldsr128f(d), v = ldsr128f(d + 1024);
#pragma unroll
    for (int i = 0; i < 4; ++i) { own[i] += u[i]; own[4 + i] += v[i]; }
  }
  const u32x4 hf = {pk(own[0] + lo16(x0[0]), own[1] + hi16(x0[0])), pk(own[2] + lo16(x0[1]), own[3] + hi16(x0[1])),
                    pk(own[4] + lo16(x1[0]), own[5] + hi16(x1[0])), pk(own[6] + lo16(x1[1]), own[7] + hi16(x1[1]))};
  *(u32x4*)(ws + HF_OFF + ((size_t)(2 * tb + tt) * 32 + 4 * c + kq) * 1024 + lane * 16) = hf;
}

constexpr int K4_ST = 65536, K4_CS = K4_ST + 512, K4_LDS = K4_CS + 8 * 128;
__global__ __launch_bounds__(512) void k_ffn1(char* __restrict__ ws) {
  extern __shared__ __attribute__((aligned(16))) char smem[];
  const unsigned lds0 = (unsigned)(uintptr_t)smem;
  const int tid = threadIdx.x, lane = tid & 63, w = RFL(tid >> 6), hh = lane >> 5;
  const int g = blockIdx.x & 7, r = blockIdx.x >> 3, tb = 4 * g + (r >> 3), c = r & 7;
  {
    const char* hb = ws + HF_OFF + (size_t)tb * 65536 + w * 1024;
#pragma unroll
    for (int i = 0; i < 8; ++i) glds(hb + i * 8192, lane * 16, lds0 + w * 1024 + i * 8192);
  }
  const unsigned aa = lds0 + lane * 16;
  if (w < 7) {
    const int ft = 7 * c + w;
    u32x4 wf[32];
#pragma unroll
    for (int i = 0; i < 32; ++i) wf[i] = *(const u32x4*)(ws + W1_OFF + ((size_t)ft * 32 + i) * 1024 + lane * 16);
    asm volatile("s_waitcnt vmcnt(32)" ::: "memory");
    BARRIER();
    f32x16 acc[2] = {};
    float cs = 0.f;
#pragma unroll
    for (int i = 0; i < 32; ++i) {
      acc[0] = mfma(wf[i], ldsr128(aa + i * 1024), acc[0]);
      acc[1] = mfma(wf[i], ldsr128(aa + 32768 + i * 1024), acc[1]);
#pragma unroll
      for (int j = 0; j < 4; ++j) cs = dot2(wf[i][j], ONES2, cs);
    }
    cs = halfsum(cs);
    const unsigned csb = lds0 + K4_CS + w * 128;
    *(LAS float*)(csb + (lane & 31) * 4) = cs;
    BARRIER();
    f32x4 cv[4];
#pragma unroll
    for (int i = 0; i < 4; ++i) cv[i] = ldsr128f(csb + (8 * i + 4 * hh) * 4);
#pragma unroll
    for (int tt = 0; tt < 2; ++tt) {
      const float rstd = *(const LAS float*)(lds0 + K4_ST + (32 * tt + (lane & 31)) * 4), nmr = *(const LAS float*)(lds0 + K4_ST + 256 + (32 * tt + (lane & 31)) * 4);
      float v[16];
#pragma unroll
      for (int i = 0; i < 16; ++i) { const float y = acc[tt][i] * rstd + nmr * cv[i >> 2][i & 3]; v[i] = y < 0.f ? 0.f : y; }
#pragma unroll
      for (int s = 0; s < 2; ++s)
        *(u32x4*)(ws + H1_OFF + ((size_t)(2 * tb + tt) * 112 + 2 * ft + s) * 1024 + lane * 16) =
            u32x4{pk(v[8 * s], v[8 * s + 1]), pk(v[8 * s + 2], v[8 * s + 3]), pk(v[8 * s + 4], v[8 * s + 5]), pk(v[8 * s + 6], v[8 * s + 7])};
    }
  } else {
    WAITV(0);
    BARRIER();
    float s0 = 0.f, s1 = 0.f, q0 = 0.f, q1 = 0.f;
#pragma unroll 8
    for (int i = 0; i < 32; ++i) {
      const u32x4 a = ldsr128(aa + i * 1024), b = ldsr128(aa + 32768 + i * 1024);
#pragma unroll
      for (int j = 0; j < 4; ++j) { s0 = dot2(a[j], ONES2, s0); q0 = dot2(a[j], a[j], q0); s1 = dot2(b[j], ONES2, s1); q1 = dot2(b[j], b[j], q1); }
    }
    s0 = halfsum(s0); s1 = halfsum(s1); q0 = halfsum(q0); q1 = halfsum(q1);
    const float sm = hh ? s1 : s0, sq = hh ? q1 : q0;
    const float mu = sm * (1.f / D), var = fmaxf(sq * (1.f / D) - mu * mu, 0.f), rstd = rsqrtf(var + LN_EPS);
    *(LAS float*)(lds0 + K4_ST + lane * 4) = rstd;
    *(LAS float*)(lds0 + K4_ST + 256 + lane * 4) = -mu * rstd;
    BARRIER();
  }
}

constexpr int K5_LDS = 4 * 32768;
__global__ __launch_bounds__(512) void k_ffn2(char* __restrict__ ws, char* __restrict__ out) {
  extern __shared__ __attribute__((aligned(16))) char smem[];
  const unsigned lds0 = (unsigned)(uintptr_t)smem;
  const int tid = threadIdx.x, lane = tid & 63, w = RFL(tid >> 6), hh = lane >> 5;
  const int g = blockIdx.x & 7, r = blockIdx.x >> 3, tb = 4 * g + (r >> 3), c = r & 7;
  const int nt = (w >> 1) & 1, kq = (w >> 2) * 2 + (w & 1);
  const char* ab = ws + H1_OFF + ((size_t)(2 * tb + (w & 1)) * 112 + 28 * (w >> 1)) * 1024;
  auto stage = [&](int st) {
#pragma unroll
    for (int i = 0; i < 4; ++i) glds(ab + (st * 4 + i) * 1024, lane * 16, lds0 + (st & 3) * 32768 + w * 4096 + i * 1024);
  };
  const char* wb = ws + W2_OFF + ((size_t)(2 * c + nt) * 112 + 28 * kq) * 1024 + lane * 16;
  u32x4 wf[28];
#define LDW(a, b) _Pragma("unroll") for (int i_ = a; i_ < b; ++i_) asm volatile("global_load_dwordx4 %0, %1, off offset:0" : "=v"(wf[i_]) : "v"(wb + i_ * 1024))
#define PINW(a, b) _Pragma("unroll") for (int i_ = a; i_ < b; ++i_) asm volatile("" : "+v"(wf[i_]))
  u32x4 hf;
  asm volatile("global_load_dwordx4 %0, %1, off offset:0" : "=v"(hf) : "v"(ws + HF_OFF + ((size_t)(2 * tb + (w >> 2)) * 32 + 4 * c + (w & 3)) * 1024 + lane * 16));
  stage(0); LDW(0, 4); stage(1); LDW(4, 8); stage(2); LDW(8, 12); stage(3); LDW(12, 16);
  f32x16 acc[2] = {};
  const unsigned aa = lds0 + kq * 8192 + lane * 16;
#pragma unroll
  for (int st = 0; st < 7; ++st) {
    if (st == 0) WAITV(24); else if (st < 5) WAITV(16); else if (st == 5) WAITV(8); else WAITV(0);
    BARRIER();
    PINW(st * 4, st * 4 + 4);
    if (st >= 1 && st + 3 < 7) { stage(st + 3); LDW(st * 4 + 12, st * 4 + 16); }
#pragma unroll
    for (int i = 0; i < 4; ++i) {
      acc[0] = mfma(wf[st * 4 + i], ldsr128(aa + (st & 3) * 32768 + i * 1024), acc[0]);
      acc[1] = mfma(wf[st * 4 + i], ldsr128(aa + (st & 3) * 32768 + 4096 + i * 1024), acc[1]);
    }
  }
  asm volatile("" : "+v"(hf));
  BARRIER();
  float own[8];
#pragma unroll
  for (int j = 0; j < 4; ++j) {
    const f32x16& a = acc[j >> 1];
    const int b = 8 * (j & 1);
    if (j == kq) {
#pragma unroll
      for (int i = 0; i < 8; ++i) own[i] = a[b + i];
    } else {
      const unsigned d = lds0 + ((nt * 4 + j) * 4 + kq) * 2048 + lane * 16;
      *(LAS f32x4*)d = f32x4{a[b], a[b + 1], a[b + 2], a[b + 3]};
      *(LAS f32x4*)(d + 1024) = f32x4{a[b + 4], a[b + 5], a[b + 6], a[b + 7]};
    }
  }
  BARRIER();
#pragma unroll
  for (int j = 1; j < 4; ++j) {
    const unsigned d = lds0 + ((nt * 4 + kq) * 4 + (kq ^ j)) * 2048 + lane * 16;
    const f32x4 u = ldsr128f(d), v = ldsr128f(d + 1024);
#pragma unroll
    for (int i = 0; i < 4; ++i) { own[i] += u[i]; own[4 + i] += v[i]; }
  }
  unsigned a0 = pk(own[0] + lo16(hf[0]), own[1] + hi16(hf[0])), a1 = pk(own[2] + lo16(hf[1]), own[3] + hi16(hf[1]));
  unsigned b0 = pk(own[4] + lo16(hf[2]), own[5] + hi16(hf[2])), b1 = pk(own[6] + lo16(hf[3]), own[7] + hi16(hf[3]));
  { auto t = __builtin_amdgcn_permlane32_swap(a0, b0, false, false); a0 = t[0]; b0 = t[1]; }
  { auto t = __builtin_amdgcn_permlane32_swap(a1, b1, false, false); a1 = t[0]; b1 = t[1]; }
  *(u32x4*)(out + (size_t)(64 * tb + 32 * (w >> 2) + (lane & 31)) * 1024 + (64 * c + 16 * (w & 3) + 8 * hh) * 2) = u32x4{a0, a1, b0, b1};
}
}

extern "C" void kernel_launch(void* const* d_in, const int* in_sizes, int n_in, void* d_out, int out_size, void* d_ws, size_t ws_size,
                              hipStream_t stream) {
  using namespace blk;
  const char* x = (const char*)d_in[0];
  const char* wqkv = (const char*)d_in[1];
  const char* wo = (const char*)d_in[2];
  const char* w1 = (const char*)d_in[4];
  const char* w2 = (const char*)d_in[5];
  char* ws = (char*)d_ws;
  (void)hipFuncSetAttribute((const void*)k_qkv, hipFuncAttributeMaxDynamicSharedMemorySize, K1_LDS);
  (void)hipFuncSetAttribute((const void*)k_attn, hipFuncAttributeMaxDynamicSharedMemorySize, K2_LDS);
  (void)hipFuncSetAttribute((const void*)k_oproj, hipFuncAttributeMaxDynamicSharedMemorySize, K3_LDS);
  (void)hipFuncSetAttribute((const void*)k_ffn1, hipFuncAttributeMaxDynamicSharedMemorySize, K4_LDS);
  (void)hipFuncSetAttribute((const void*)k_ffn2, hipFuncAttributeMaxDynamicSharedMemorySize, K5_LDS);
  k_qkv<<<256, 512, K1_LDS, stream>>>(x, wqkv, wo, w1, w2, ws);
  k_attn<<<256, 512, K2_LDS, stream>>>(ws);
  k_oproj<<<256, 512, K3_LDS, stream>>>(x, ws);
  k_ffn1<<<256, 512, K4_LDS, stream>>>(ws);
  k_ffn2<<<256, 512, K5_LDS, stream>>>(ws, (char*)d_out);
}
```

```cpp
  f32x16 sa = mfma(ldsr128(aK), qf, nm), sb = mfma(ldsr128(aK + TS), qf, nm);
  u32x4 kf = ldsr128(aK + 2 * TS), v0 = ldsr128(aV0), v1 = ldsr128(aV1);
#pragma unroll
  for (int i = 0; i < 16; ++i) sa[i] = __builtin_amdgcn_exp2f(sa[i]);
  unsigned nit = 8, idx = 0xDDDD, a0 = aK, a1 = aV0, a2 = aV1;
  asm volatile("s_nop 1\n\t1:\n\ts_waitcnt lgkmcnt(2)\n\tv_smfmac_f32_32x32x32_bf16 v[0:15], v[76:79], v[32:39], v68\n\tds_read_b128 v[76:79], v70 offset:2064\n\tv_exp_f32 v48, v48\n\tv_exp_f32 v49, v49\n\tv_exp_f32 v50, v50\n\ts_waitcnt lgkmcnt(2)\n\tv_smfmac_f32_32x32x32_bf16 v[0:15], v[80:83], v[40:47], v68\n\tds_read_b128 v[80:83], v71 offset:2064\n\tv_exp_f32 v51, v51\n\tv_exp_f32 v52, v52\n\tv_exp_f32 v53, v53\n\ts_waitcnt lgkmcnt(2)\n\tv_mfma_f32_32x32x16_bf16 v[32:47], v[72:75], v[64:67], v[16:31]\n\tds_read_b128 v[72:75], v69 offset:6192\n\tv_exp_f32 v54, v54\n\tv_exp_f32 v55, v55\n\tv_exp_f32 v56, v56\n\tv_exp_f32 v57, v57\n\tv_exp_f32 v58, v58\n\tv_exp_f32 v59, v59\n\tv_exp_f32 v60, v60\n\tv_exp_f32 v61, v61\n\tv_exp_f32 v62, v62\n\tv_exp_f32 v63, v63\n\ts_waitcnt lgkmcnt(2)\n\tv_smfmac_f32_32x32x32_bf16 v[0:15], v[76:79], v[48:55], v68\n\tds_read_b128 v[76:79], v70 offset:4128\n\tv_exp_f32 v32, v32\n\tv_exp_f32 v33, v33\n\tv_exp_f32 v34, v34\n\ts_waitcnt lgkmcnt(2)\n\tv_smfmac_f32_32x32x32_bf16 v[0:15], v[80:83], v[56:63], v68\n\tds_read_b128 v[80:83], v71 offset:4128\n\tv_exp_f32 v35, v35\n\tv_exp_f32 v36, v36\n\tv_exp_f32 v37, v37\n\ts_waitcnt lgkmcnt(2)\n\tv_mfma_f32_32x32x16_bf16 v[48:63], v[72:75], v[64:67], v[16:31]\n\tds_read_b128 v[72:75], v69 offset:8256\n\tv_exp_f32 v38, v38\n\tv_exp_f32 v39, v39\n\tv_exp_f32 v40, v40\n\tv_exp_f32 v41, v41\n\tv_exp_f32 v42, v42\n\tv_exp_f32 v43, v43\n\tv_exp_f32 v44, v44\n\tv_exp_f32 v45, v45\n\tv_exp_f32 v46, v46\n\tv_exp_f32 v47, v47\n\ts_waitcnt lgkmcnt(2)\n\tv_smfmac_f32_32x32x32_bf16 v[0:15], v[76:79], v[32:39], v68\n\tds_read_b128 v[76:79], v70 offset:6192\n\tv_exp_f32 v48, v48\n\tv_exp_f32 v49, v49\n\tv_exp_f32 v50, v50\n\ts_waitcnt lgkmcnt(2)\n\tv_smfmac_f32_32x32x32_bf16 v[0:15], v[80:83], v[40:47], v68\n\tds_read_b128 v[80:83], v71 offset:6192\n\tv_exp_f32 v51, v51\n\tv_exp_f32 v52, v52\n\tv_exp_f32 v53, v53\n\ts_waitcnt lgkmcnt(2)\n\tv_mfma_f32_32x32x16_bf16 v[32:47], v[72:75], v[64:67], v[16:31]\n\tds_read_b128 v[72:75], v69 offset:10320\n\tv_exp_f32 v54, v54\n\tv_exp_f32 v55, v55\n\tv_exp_f32 v56, v56\n\tv_exp_f32 v57, v57\n\tv_exp_f32 v58, v58\n\tv_exp_f32 v59, v59\n\tv_exp_f32 v60, v60\n\tv_exp_f32 v61, v61\n\tv_exp_f32 v62, v62\n\tv_exp_f32 v63, v63\n\ts_waitcnt lgkmcnt(2)\n\tv_smfmac_f32_32x32x32_bf16 v[0:15], v[76:79], v[48:55], v68\n\tds_read_b128 v[76:79], v70 offset:8256\n\tv_exp_f32 v32, v32\n\tv_exp_f32 v33, v33\n\tv_exp_f32 v34, v34\n\ts_waitcnt lgkmcnt(2)\n\tv_smfmac_f32_32x32x32_bf16 v[0:15], v[80:83], v[56:63], v68\n\tds_read_b128 v[80:83], v71 offset:8256\n\tv_exp_f32 v35, v35\n\tv_exp_f32 v36, v36\n\tv_exp_f32 v37, v37\n\ts_waitcnt lgkmcnt(2)\n\tv_mfma_f32_32x32x16_bf16 v[48:63], v[72:75], v[64:67], v[16:31]\n\tds_read_b128 v[72:75], v69 offset:12384\n\tv_exp_f32 v38, v38\n\tv_exp_f32 v39, v39\n\tv_exp_f32 v40, v40\n\tv_exp_f32 v41, v41\n\tv_exp_f32 v42, v42\n\tv_exp_f32 v43, v43\n\tv_exp_f32 v44, v44\n\tv_exp_f32 v45, v45\n\tv_exp_f32 v46, v46\n\tv_exp_f32 v47, v47\n\ts_waitcnt lgkmcnt(2)\n\tv_smfmac_f32_32x32x32_bf16 v[0:15], v[76:79], v[32:39], v68\n\tds_read_b128 v[76:79], v70 offset:10320\n\tv_exp_f32 v48, v48\n\tv_exp_f32 v49, v49\n\tv_exp_f32 v50, v50\n\ts_waitcnt lgkmcnt(2)\n\tv_smfmac_f32_32x32x32_bf16 v[0:15], v[80:83], v[40:47], v68\n\tds_read_b128 v[80:83], v71 offset:10320\n\tv_exp_f32 v51, v51\n\tv_exp_f32 v52, v52\n\tv_exp_f32 v53, v53\n\ts_waitcnt lgkmcnt(2)\n\tv_mfma_f32_32x32x16_bf16 v[32:47], v[72:75], v[64:67], v[16:31]\n\tds_read_b128 v[72:75], v69 offset:14448\n\tv_exp_f32 v54, v54\n\tv_exp_f32 v55, v55\n\tv_exp_f32 v56, v56\n\tv_exp_f32 v57, v57\n\tv_exp_f32 v58, v58\n\tv_exp_f32 v59, v59\n\tv_exp_f32 v60, v60\n\tv_exp_f32 v61, v61\n\tv_exp_f32 v62, v62\n\tv_exp_f32 v63, v63\n\ts_waitcnt lgkmcnt(2)\n\tv_smfmac_f32_32x32x32_bf16 v[0:15], v[76:79], v[48:55], v68\n\tds_read_b128 v[76:79], v70 offset:12384\n\tv_exp_f32 v32, v32\n\tv_exp_f32 v33, v33\n\tv_exp_f32 v34, v34\n\ts_waitcnt lgkmcnt(2)\n\tv_smfmac_f32_32x32x32_bf16 v[0:15], v[80:83], v[56:63], v68\n\tds_read_b128 v[80:83], v71 offset:12384\n\tv_exp_f32 v35, v35\n\tv_exp_f32 v36, v36\n\tv_exp_f32 v37, v37\n\ts_waitcnt lgkmcnt(2)\n\tv_mfma_f32_32x32x16_bf16 v[48:63], v[72:75], v[64:67], v[16:31]\n\tds_read_b128 v[72:75], v69 offset:16512\n\tv_exp_f32 v38, v38\n\tv_exp_f32 v39, v39\n\tv_exp_f32 v40, v40\n\tv_exp_f32 v41, v41\n\tv_exp_f32 v42, v42\n\tv_exp_f32 v43, v43\n\tv_exp_f32 v44, v44\n\tv_exp_f32 v45, v45\n\tv_exp_f32 v46, v46\n\tv_exp_f32 v47, v47\n\ts_waitcnt lgkmcnt(2)\n\tv_smfmac_f32_32x32x32_bf16 v[0:15], v[76:79], v[32:39], v68\n\tds_read_b128 v[76:79], v70 offset:14448\n\tv_exp_f32 v48, v48\n\tv_exp_f32 v49, v49\n\tv_exp_f32 v50, v50\n\ts_waitcnt lgkmcnt(2)\n\tv_smfmac_f32_32x32x32_bf16 v[0:15], v[80:83], v[40:47], v68\n\tds_read_b128 v[80:83], v71 offset:14448\n\tv_exp_f32 v51, v51\n\tv_exp_f32 v52, v52\n\tv_exp_f32 v53, v53\n\ts_waitcnt lgkmcnt(2)\n\tv_mfma_f32_32x32x16_bf16 v[32:47], v[72:75], v[64:67], v[16:31]\n\tds_read_b128 v[72:75], v69 offset:18576\n\tv_exp_f32 v54, v54\n\tv_exp_f32 v55, v55\n\tv_exp_f32 v56, v56\n\tv_exp_f32 v57, v57\n\tv_exp_f32 v58, v58\n\tv_exp_f32 v59, v59\n\tv_exp_f32 v60, v60\n\tv_exp_f32 v61, v61\n\tv_exp_f32 v62, v62\n\tv_exp_f32 v63, v63\n\ts_sub_u32 %[n], %[n], 1\n\ts_cmp_eq_u32 %[n], 0\n\ts_waitcnt lgkmcnt(2)\n\tv_smfmac_f32_32x32x32_bf16 v[0:15], v[76:79], v[48:55], v68\n\tds_read_b128 v[76:79], v70 offset:16512\n\tv_exp_f32 v32, v32\n\tv_exp_f32 v33, v33\n\tv_exp_f32 v34, v34\n\ts_waitcnt lgkmcnt(2)\n\tv_smfmac_f32_32x32x32_bf16 v[0:15], v[80:83], v[56:63], v68\n\ts_cbranch_scc1 2f\n\tds_read_b128 v[80:83], v71 offset:16512\n\tv_exp_f32 v35, v35\n\tv_exp_f32 v36, v36\n\tv_exp_f32 v37, v37\n\ts_waitcnt lgkmcnt(2)\n\tv_mfma_f32_32x32x16_bf16 v[48:63], v[72:75], v[64:67], v[16:31]\n\tds_read_b128 v[72:75], v69 offset:20640\n\tv_exp_f32 v38, v38\n\tv_exp_f32 v39, v39\n\tv_exp_f32 v40, v40\n\tv_exp_f32 v41, v41\n\tv_exp_f32 v42, v42\n\tv_exp_f32 v43, v43\n\tv_exp_f32 v44, v44\n\tv_exp_f32 v45, v45\n\tv_exp_f32 v46, v46\n\tv_exp_f32 v47, v47\n\tv_add_u32 v69, 0x4080, v69\n\tv_add_u32 v70, 0x4080, v70\n\tv_add_u32 v71, 0x4080, v71\n\ts_branch 1b\n\t2:\n\ts_waitcnt lgkmcnt(0)\n\ts_nop 11"
     : "+{v[0:15]}"(o), "+{v[32:47]}"(sa), "+{v[48:63]}"(sb), "+{v[72:75]}"(kf), "+{v[76:79]}"(v0), "+{v[80:83]}"(v1), "+{v69}"(a0), "+{v70}"(a1), "+{v71}"(a2), [n] "+s"(nit)
     : "{v[16:31]}"(nm), "{v[64:67]}"(qf), "{v68}"(idx) : "scc", "memory");
  const float inv = 1.f / (hh ? o[12] : o[8]);
  asm volatile("" :: "v"(inv));
  STAMPL(2);
  *(u32x4*)(ws + OF_OFF + ((size_t)qt * 32 + h) * 1024 + lane * 16) =
      u32x4{pk(o[0] * inv, o[1] * inv), pk(o[2] * inv, o[3] * inv), pk(o[4] * inv, o[5] * inv), pk(o[6] * inv, o[7] * inv)};
  WAITV(0); STAMPL(3); STAMPOUT(1);
}

constexpr int K3_LDS = 8 * 4 * 2048;
__global__ __launch_bounds__(512) void k_oproj(const char* __restrict__ x, char* __restrict__ ws) {
  extern __shared__ __attribute__((aligned(16))) char smem[];
  const unsigned lds0 = (unsigned)(uintptr_t)smem;
  STAMPDECL;
  const int tid = threadIdx.x, lane = tid & 63, w = RFL(tid >> 6), hh = lane >> 5;
  const int g = blockIdx.x & 7, r = blockIdx.x >> 3, tb = 4 * g + (r >> 3), c = r & 7;
  const int tt = w >> 2, kq = w & 3;
  STAMPL(0);
  u32x4 wf[2][8], af[8];
#pragma unroll
  for (int i = 0; i < 8; ++i) af[i] = *(const u32x4*)(ws + OF_OFF + ((size_t)(2 * tb + tt) * 32 + 8 * kq + i) * 1024 + lane * 16);
#pragma unroll
  for (int i = 0; i < 8; ++i)
#pragma unroll
    for (int n = 0; n < 2; ++n) wf[n][i] = *(const u32x4*)(ws + WO_OFF + ((size_t)(2 * c + n) * 32 + 8 * kq + i) * 1024 + lane * 16);
  const char* xp = x + (size_t)(64 * tb + 32 * tt + (lane & 31)) * 1024 + (64 * c + 16 * kq + 4 * hh) * 2;
  const u32x2 x0 = *(const u32x2*)xp, x1 = *(const u32x2*)(xp + 16);
  f32x16 acc[2] = {};
#pragma unroll
  for (int i = 0; i < 8; ++i) { acc[0] = mfma(wf[0][i], af[i], acc[0]); acc[1] = mfma(wf[1][i], af[i], acc[1]); }
  asm volatile("" :: "v"(acc[0]), "v"(acc[1])); STAMPL(1);
  float own[8];
#pragma unroll
  for (int j = 0; j < 4; ++j) {
    const f32x16& a = acc[j >> 1];
    const int b = 8 * (j & 1);
    if (j == kq) {
#pragma unroll
      for (int i = 0; i < 8; ++i) own[i] = a[b + i];
    } else {
      const unsigned d = lds0 + ((tt * 4 + j) * 4 + kq) * 2048 + lane * 16;
      *(LAS f32x4*)d = f32x4{a[b], a[b + 1], a[b + 2], a[b + 3]};
      *(LAS f32x4*)(d + 1024) = f32x4{a[b + 4], a[b + 5], a[b + 6], a[b + 7]};
    }
  }
  BARRIER();
#pragma unroll
  for (int j = 1; j < 4; ++j) {
    const unsigned d = lds0 + (w * 4 + (kq ^ j)) * 2048 + lane * 16;
    const f32x4 u = ldsr128f(d), v = ldsr128f(d + 1024);
#pragma unroll
    for (int i = 0; i < 4; ++i) { own[i] += u[i]; own[4 + i] += v[i]; }
  }
  const u32x4 hf = {pk(own[0] + lo16(x0[0]), own[1] + hi16(x0[0])), pk(own[2] + lo16(x0[1]), own[3] + hi16(x0[1])),
                    pk(own[4] + lo16(x1[0]), own[5] + hi16(x1[0])), pk(own[6] + lo16(x1[1]), own[7] + hi16(x1[1]))};
  *(u32x4*)(ws + HF_OFF + ((size_t)(2 * tb + tt) * 32 + 4 * c + kq) * 1024 + lane * 16) = hf;
  STAMPL(2); WAITV(0); STAMPL(3); STAMPOUT(2);
}

constexpr int K4_ST = 65536, K4_CS = K4_ST + 512, K4_LDS = K4_CS + 8 * 128;
__global__ __launch_bounds__(512) void k_ffn1(char* __restrict__ ws) {
  extern __shared__ __attribute__((aligned(16))) char smem[];
  const unsigned lds0 = (unsigned)(uintptr_t)smem;
  STAMPDECL;
  const int tid = threadIdx.x, lane = tid & 63, w = RFL(tid >> 6), hh = lane >> 5;
  const int g = blockIdx.x & 7, r = blockIdx.x >> 3, tb = 4 * g + (r >> 3), c = r & 7;
  STAMPL(0);
  {
    const char* hb = ws + HF_OFF + (size_t)tb * 65536 + w * 1024;
#pragma unroll
    for (int i = 0; i < 8; ++i) glds(hb + i * 8192, lane * 16, lds0 + w * 1024 + i * 8192);
  }
  const unsigned aa = lds0 + lane * 16;
  if (w < 7) {
    const int ft = 7 * c + w;
    u32x4 wf[32];
#pragma unroll
    for (int i = 0; i < 32; ++i) wf[i] = *(const u32x4*)(ws + W1_OFF + ((size_t)ft * 32 + i) * 1024 + lane * 16);
    asm volatile("s_waitcnt vmcnt(32)" ::: "memory");
    BARRIER(); STAMPL(1);
    f32x16 acc[2] = {};
    float cs = 0.f;
#pragma unroll
    for (int i = 0; i < 32; ++i) {
      acc[0] = mfma(wf[i], ldsr128(aa + i * 1024), acc[0]);
      acc[1] = mfma(wf[i], ldsr128(aa + 32768 + i * 1024), acc[1]);
#pragma unroll
      for (int j = 0; j < 4; ++j) cs = dot2(wf[i][j], ONES2, cs);
    }
    cs = halfsum(cs);
    const unsigned csb = lds0 + K4_CS + w * 128;
    *(LAS float*)(csb + (lane & 31) * 4) = cs;
    STAMPL(2);
    BARRIER(); STAMPL(3);
    f32x4 cv[4];
#pragma unroll
    for (int i = 0; i < 4; ++i) cv[i] = ldsr128f(csb + (8 * i + 4 * hh) * 4);
#pragma unroll
    for (int tt = 0; tt < 2; ++tt) {
      const float rstd = *(const LAS float*)(lds0 + K4_ST + (32 * tt + (lane & 31)) * 4), nmr = *(const LAS float*)(lds0 + K4_ST + 256 + (32 * tt + (lane & 31)) * 4);
      float v[16];
#pragma unroll
      for (int i = 0; i < 16; ++i) { const float y = acc[tt][i] * rstd + nmr * cv[i >> 2][i & 3]; v[i] = y < 0.f ? 0.f : y; }
#pragma unroll
      for (int s = 0; s < 2; ++s)
        *(u32x4*)(ws + H1_OFF + ((size_t)(2 * tb + tt) * 112 + 2 * ft + s) * 1024 + lane * 16) =
            u32x4{pk(v[8 * s], v[8 * s + 1]), pk(v[8 * s + 2], v[8 * s + 3]), pk(v[8 * s + 4], v[8 * s + 5]), pk(v[8 * s + 6], v[8 * s + 7])};
    }
    STAMPL(4); WAITV(0); STAMPL(5); STAMPOUT(3);
  } else {
    WAITV(0);
    BARRIER();
    float s0 = 0.f, s1 = 0.f, q0 = 0.f, q1 = 0.f;
#pragma unroll 8
    for (int i = 0; i < 32; ++i) {
      const u32x4 a = ldsr128(aa + i * 1024), b = ldsr128(aa + 32768 + i * 1024);
#pragma unroll
      for (int j = 0; j < 4; ++j) { s0 = dot2(a[j], ONES2, s0); q0 = dot2(a[j], a[j], q0); s1 = dot2(b[j], ONES2, s1); q1 = dot2(b[j], b[j], q1); }
    }
    s0 = halfsum(s0); s1 = halfsum(s1); q0 = halfsum(q0); q1 = halfsum(q1);
    const float sm = hh ? s1 : s0, sq = hh ? q1 : q0;
    const float mu = sm * (1.f / D), var = fmaxf(sq * (1.f / D) - mu * mu, 0.f), rstd = rsqrtf(var + LN_EPS);
    *(LAS float*)(lds0 + K4_ST + lane * 4) = rstd;
    *(LAS float*)(lds0 + K4_ST + 256 + lane * 4) = -mu * rstd;
    BARRIER();
  }
}

constexpr int K5_LDS = 4 * 32768;
__global__ __launch_bounds__(512) void k_ffn2(char* __restrict__ ws, char* __restrict__ out) {
  extern __shared__ __attribute__((aligned(16))) char smem[];
  const unsigned lds0 = (unsigned)(uintptr_t)smem;
  STAMPDECL;
  const int tid = threadIdx.x, lane = tid & 63, w = RFL(tid >> 6), hh = lane >> 5;
  const int g = blockIdx.x & 7, r = blockIdx.x >> 3, tb = 4 * g + (r >> 3), c = r & 7;
  const int nt = (w >> 1) & 1, kq = (w >> 2) * 2 + (w & 1);
  const char* ab = ws + H1_OFF + ((size_t)(2 * tb + (w & 1)) * 112 + 28 * (w >> 1)) * 1024;
  auto stage = [&](int st) {
#pragma unroll
    for (int i = 0; i < 4; ++i) glds(ab + (st * 4 + i) * 1024, lane * 16, lds0 + (st & 3) * 32768 + w * 4096 + i * 1024);
  };
  const char* wb = ws + W2_OFF + ((size_t)(2 * c + nt) * 112 + 28 * kq) * 1024 + lane * 16;
  u32x4 wf[28];
#define LDW(a, b) _Pragma("unroll") for (int i_ = a; i_ < b; ++i_) asm volatile("global_load_dwordx4 %0, %1, off offset:0" : "=v"(wf[i_]) : "v"(wb + i_ * 1024))
#define PINW(a, b) _Pragma("unroll") for (int i_ = a; i_ < b; ++i_) asm volatile("" : "+v"(wf[i_]))
  STAMPL(0);
  u32x4 hf;
  asm volatile("global_load_dwordx4 %0, %1, off offset:0" : "=v"(hf) : "v"(ws + HF_OFF + ((size_t)(2 * tb + (w >> 2)) * 32 + 4 * c + (w & 3)) * 1024 + lane * 16));
  stage(0); LDW(0, 4); stage(1); LDW(4, 8); stage(2); LDW(8, 12); stage(3); LDW(12, 16);
  f32x16 acc[2] = {};
  const unsigned aa = lds0 + kq * 8192 + lane * 16;
#pragma unroll
  for (int st = 0; st < 7; ++st) {
    if (st == 0) WAITV(24); else if (st < 5) WAITV(16); else if (st == 5) WAITV(8); else WAITV(0);
    BARRIER();
    PINW(st * 4, st * 4 + 4); if (st == 0) STAMPL(1); if (st == 3) STAMPL(2);
    if (st >= 1 && st + 3 < 7) { stage(st + 3); LDW(st * 4 + 12, st * 4 + 16); }
#pragma unroll
    for (int i = 0; i < 4; ++i) {
      acc[0] = mfma(wf[st * 4 + i], ldsr128(aa + (st & 3) * 32768 + i * 1024), acc[0]);
      acc[1] = mfma(wf[st * 4 + i], ldsr128(aa + (st & 3) * 32768 + 4096 + i * 1024), acc[1]);
    }
  }
  asm volatile("" : "+v"(hf));
  asm volatile("" :: "v"(acc[0]), "v"(acc[1])); STAMPL(3);
  BARRIER();
  float own[8];
#pragma unroll
  for (int j = 0; j < 4; ++j) {
    const f32x16& a = acc[j >> 1];
    const int b = 8 * (j & 1);
    if (j == kq) {
#pragma unroll
      for (int i = 0; i < 8; ++i) own[i] = a[b + i];
    } else {
      const unsigned d = lds0 + ((nt * 4 + j) * 4 + kq) * 2048 + lane * 16;
      *(LAS f32x4*)d = f32x4{a[b], a[b + 1], a[b + 2], a[b + 3]};
      *(LAS f32x4*)(d + 1024) = f32x4{a[b + 4], a[b + 5], a[b + 6], a[b + 7]};
    }
  }
  BARRIER();
#pragma unroll
  for (int j = 1; j < 4; ++j) {
    const unsigned d = lds0 + ((nt * 4 + kq) * 4 + (kq ^ j)) * 2048 + lane * 16;
    const f32x4 u = ldsr128f(d), v = ldsr128f(d + 1024);
#pragma unroll
    for (int i = 0; i < 4; ++i) { own[i] += u[i]; own[4 + i] += v[i]; }
  }
  unsigned a0 = pk(own[0] + lo16(hf[0]), own[1] + hi16(hf[0])), a1 = pk(own[2] + lo16(hf[1]), own[3] + hi16(hf[1]));
  unsigned b0 = pk(own[4] + lo16(hf[2]), own[5] + hi16(hf[2])), b1 = pk(own[6] + lo16(hf[3]), own[7] + hi16(hf[3]));
  { auto t = __builtin_amdgcn_permlane32_swap(a0, b0, false, false); a0 = t[0]; b0 = t[1]; }
  { auto t = __builtin_amdgcn_permlane32_swap(a1, b1, false, false); a1 = t[0]; b1 = t[1]; }
  *(u32x4*)(out + (size_t)(64 * tb + 32 * (w >> 2) + (lane & 31)) * 1024 + (64 * c + 16 * (w & 3) + 8 * hh) * 2) = u32x4{a0, a1, b0, b1};
  STAMPL(4); WAITV(0); STAMPL(5); STAMPOUT(4);
}

}

extern "C" void kernel_launch(void* const* d_in, const int* in_sizes, int n_in, void* d_out, int out_size, void* d_ws, size_t ws_size,
                              hipStream_t stream) {
  using namespace blk;
  const char* x = (const char*)d_in[0];
  const char* wqkv = (const char*)d_in[1];
  const char* wo = (const char*)d_in[2];
  const char* w1 = (const char*)d_in[4];
  const char* w2 = (const char*)d_in[5];
  char* ws = (char*)d_ws;
  (void)hipFuncSetAttribute((const void*)k_qkv, hipFuncAttributeMaxDynamicSharedMemorySize, K1_LDS);
  (void)hipFuncSetAttribute((const void*)k_attn, hipFuncAttributeMaxDynamicSharedMemorySize, K2_LDS);
  (void)hipFuncSetAttribute((const void*)k_oproj, hipFuncAttributeMaxDynamicSharedMemorySize, K3_LDS);
  (void)hipFuncSetAttribute((const void*)k_ffn1, hipFuncAttributeMaxDynamicSharedMemorySize, K4_LDS);
  (void)hipFuncSetAttribute((const void*)k_ffn2, hipFuncAttributeMaxDynamicSharedMemorySize, K5_LDS);
  k_qkv<<<256, 512, K1_LDS, stream>>>(x, wqkv, wo, w1, w2, ws);
  k_attn<<<256, 512, K2_LDS, stream>>>(ws);
  k_oproj<<<256, 512, K3_LDS, stream>>>(x, ws);
  k_ffn1<<<256, 512, K4_LDS, stream>>>(ws);
  k_ffn2<<<256, 512, K5_LDS, stream>>>(ws, (char*)d_out);

}
```

```cpp
#include <hip/hip_runtime.h>
#include <stdint.h>

__global__ __launch_bounds__(512, 2) void k_block(const void* x, const void* wqkv, const void* wo, const void* w1, const void* w2, void* out, void* ws) {
  unsigned tid = threadIdx.x;
  asm volatile(R"ASM(
.Lk_start:
	s_load_dwordx8 s[32:39], s[0:1], 0
	s_load_dwordx4 s[40:43], s[0:1], 32
	s_load_dwordx2 s[44:45], s[0:1], 48
	v_lshrrev_b32 v1, 6, v0
	s_getpc_b64 s[24:25]
.Lk_pc:
	v_readfirstlane_b32 s3, v1
	s_lshr_b32 s48, s2, 3
	s_mov_b32 s26, .Lk_end-.Lk_pc
	s_mov_b32 s27, 0x20000
	s_and_b32 s25, s25, 0xffff
	s_lshl_b32 s49, s48, 3
	s_add_u32 s49, s49, s3
	s_lshl_b32 s49, s49, 7
	buffer_load_dword v127, off, s[24:27], s49
	v_lshrrev_b32 v2, 3, v0
	v_and_b32 v3, 7, v0
	v_bfe_u32 v4, v0, 4, 2
	v_xor_b32 v4, v4, v3
	v_lshlrev_b32 v4, 4, v4
	v_lshl_or_b32 v4, v2, 10, v4
	s_lshl_b32 s50, s2, 2
	s_and_b32 s51, s50, 24
	s_and_b32 s52, s48, 7
	s_or_b32 s28, s51, s52
	v_xor_b32 v7, 64, v4
	s_lshl_b32 s54, s28, 16
	s_lshl_b32 s30, s3, 13
	s_add_u32 s30, s30, s54
	v_add_u32 v7, 7168, v7
	s_and_b32 s57, s3, 3
	s_lshl_b32 s57, s57, 11
	s_lshr_b32 s53, s48, 2
	s_and_b32 s53, s53, 2
	s_add_u32 s53, s53, s50
	s_and_b32 s59, s2, 6
	s_add_u32 s53, s53, s59
	s_mov_b32 s6, 0x200000
	s_mov_b32 s7, 0x20000
	s_mov_b32 m0, s57
	s_cmp_ge_u32 s3, 4
	s_waitcnt lgkmcnt(0)
	s_mov_b32 s4, s32
	s_cbranch_scc1 .Lk_nox
	s_and_b32 s5, s33, 0xffff
	s_add_u32 s56, s53, 0
	s_and_b32 s56, s56, 7
	s_mul_i32 s56, s56, 128
	s_add_u32 s56, s56, s30
	buffer_load_dwordx4 v4, s[4:7], s56 offen lds
	buffer_load_dwordx4 v7, s[4:7], s56 offen offset:1024 lds
	s_add_u32 m0, s57, 8192
	s_add_u32 s56, s53, 1
	s_and_b32 s56, s56, 7
	s_mul_i32 s56, s56, 128
	s_add_u32 s56, s56, s30
	buffer_load_dwordx4 v4, s[4:7], s56 offen lds
	buffer_load_dwordx4 v7, s[4:7], s56 offen offset:1024 lds
.Lk_nox:
	s_and_b32 s5, s33, 0xffff
	s_and_b32 s51, s50, 4
	s_lshr_b32 s59, s48, 3
	s_or_b32 s29, s51, s59
	s_mov_b32 s14, 20971520
	s_mov_b32 s15, 0x20000
	s_mov_b32 s12, s44
	s_and_b32 s13, s45, 0xffff
	s_mov_b32 s8, s34
	s_and_b32 s9, s35, 0xffff
	s_mov_b32 s10, 0x180000
	s_mov_b32 s11, 0x20000
	s_lshl_b32 s55, s29, 7
	v_lshrrev_b32 v5, 2, v0
	v_bitop3_b32 v5, v5, v3, 4 bitop3:0x6c
	v_and_b32 v6, 31, v2
	v_mul_u32_u24 v6, 0xc00, v6
	v_lshl_or_b32 v6, v5, 4, v6
	s_and_b32 s50, s3, 3
	s_lshl_b32 s62, s50, 10
	v_and_b32 v126, 63, v0
	v_lshlrev_b32 v121, 4, v126
	v_lshrrev_b32 v8, 5, v126
	s_mov_b32 s19, 0x20000
	s_mov_b32 s20, s42
	s_and_b32 s21, s43, 0xffff
	s_mov_b32 s22, 0x200000
	s_mov_b32 s23, 0x20000
	s_mov_b32 s46, 0xffff
	s_mov_b32 s47, 0xffff
	s_mov_b32 s61, 2097152
	s_mov_b32 s49, 0x3b000000
	s_cmp_ge_u32 s3, 4
	s_cbranch_scc1 .Lk_G
	s_mov_b32 s31, 20250624
	s_cmp_eq_u32 s50, 3
	s_cselect_b32 s31, s31, 1879048192
	s_lshl_b32 s58, s52, 7
	s_add_u32 s58, s58, s31
	s_and_b32 s59, s2, 0xc7
	s_cmp_eq_u32 s59, 2
	s_cselect_b32 s58, s58, 1879048192
	s_min_u32 s58, s58, 1879048192
	v_lshlrev_b32 v34, 7, v3
	buffer_load_dword v32, v34, s[12:15], s31 offen sc1
	s_lshl_b32 s50, s28, 2
	s_add_u32 s50, s50, s3
	s_mov_b32 s18, 0x1c0000
	v_lshrrev_b32 v15, 3, v126
	s_cmp_ge_u32 s50, 64
	s_cbranch_scc1 .Lk_jw2
	s_lshr_b32 s51, s50, 4
	s_and_b32 s59, s50, 15
	s_lshl_b32 s35, s29, 2
	s_add_u32 s51, s51, s35
	s_mul_i32 s35, s59, 114688
	s_lshl_b32 s32, s51, 7
	s_add_u32 s35, s35, s32
	s_lshl_b32 s32, s51, 16
	s_lshl_b32 s59, s59, 11
	s_add_u32 s32, s32, s59
	s_add_u32 s32, s32, 15728640
	s_mov_b64 s[16:17], s[38:39]
	s_movk_i32 s63, 3584
	s_cmp_eq_u32 s29, 7
	s_cselect_b32 s18, 0, s18
	s_cselect_b32 s32, 1879048192, s32
	s_add_u32 s33, s32, 0x8000
	s_branch .Lk_jdone
.Lk_jw2:
	s_sub_u32 s59, s50, 64
	s_lshl_b32 s35, s59, 8
	s_add_u32 s35, s35, s29
	s_lshl_b32 s35, s35, 7
	s_mul_i32 s32, s29, 229376
	s_and_b32 s51, s59, 7
	s_mul_i32 s51, s51, 7
	s_lshr_b32 s59, s59, 3
	s_sub_u32 s59, s59, s29
	s_cselect_b32 s63, 7, 0
	s_add_u32 s59, s59, s63
	s_add_u32 s59, s59, s51
	s_lshl_b32 s59, s59, 12
	s_add_u32 s32, s32, s59
	s_add_u32 s32, s32, 17825792
	s_mov_b64 s[16:17], s[40:41]
	s_movk_i32 s63, 1024
	s_cmp_ge_u32 s50, 120
	s_cselect_b32 s32, 1879048192, s32
	s_add_u32 s33, s32, 2048
.Lk_jdone:
	s_and_b32 s17, s17, 0xffff
	v_mul_u32_u24 v15, s63, v15
	v_lshl_or_b32 v18, v5, 4, v15
	v_and_b32 v10, 31, v0
	v_mul_u32_u24 v10, s63, v10
	s_lshl_b32 s63, s63, 3
	v_bfe_u32 v13, v0, 1, 1
	v_bfe_u32 v14, v0, 4, 1
	v_lshl_or_b32 v13, v14, 1, v13
	v_bfe_u32 v14, v0, 3, 1
	v_lshl_or_b32 v13, v14, 2, v13
	v_lshlrev_b32 v13, 4, v13
	v_and_b32 v14, 1, v0
	v_lshl_or_b32 v13, v14, 3, v13
	v_bfe_u32 v14, v0, 2, 2
	v_lshl_or_b32 v13, v14, 7, v13
	s_add_u32 s59, s62, 8192
	v_lshl_or_b32 v15, v8, 9, v13
	v_add_u32 v44, s59, v15
	v_xor_b32 v45, 64, v44
	s_mov_b64 s[24:25], s[32:33]
	v_and_b32 v23, 15, v0
	v_bfe_u32 v22, v0, 1, 3
	v_bfe_u32 v24, v0, 4, 2
	v_xor_b32 v22, v22, v24
	v_lshlrev_b32 v22, 4, v22
	v_lshl_or_b32 v22, v23, 7, v22
	v_add_u32 v22, s57, v22
	v_xor_b32 v25, 64, v22
	v_lshl_add_u32 v23, s3, 4, v23
	v_lshlrev_b32 v24, 2, v23
	v_lshlrev_b32 v23, 3, v23
	v_mov_b32 v28, 0x3f803f80
	v_mov_b32 v29, 0x3f803f80
	v_mov_b32 v30, 0x3f803f80
	v_mov_b32 v31, 0x3f803f80
	s_sleep 6
	s_add_u32 m0, s57, 16384
	s_add_u32 s56, s53, 2
	s_and_b32 s56, s56, 7
	s_mul_i32 s56, s56, 128
	s_add_u32 s56, s56, s30
	buffer_load_dwordx4 v4, s[4:7], s56 offen lds
	buffer_load_dwordx4 v7, s[4:7], s56 offen offset:1024 lds
	s_add_u32 m0, s57, 98304
	s_add_u32 s56, s53, 2
	s_and_b32 s56, s56, 7
	s_mul_i32 s56, s56, 196608
	s_add_u32 s56, s56, s55
	s_add_u32 s56, s56, 1024
	buffer_load_dwordx4 v6, s[8:11], s56 offen lds
	buffer_load_dwordx4 v6, s[8:11], s56 offen offset:1024 lds
	s_add_u32 m0, s57, 106496
	s_add_u32 s56, s56, 98304
	buffer_load_dwordx4 v6, s[8:11], s56 offen lds
	buffer_load_dwordx4 v6, s[8:11], s56 offen offset:1024 lds
	s_waitcnt vmcnt(9)
	ds_read_b128 v[128:131], v22
	ds_read_b128 v[132:135], v25
	s_barrier
	s_add_u32 m0, s57, 24576
	s_add_u32 s56, s53, 3
	s_and_b32 s56, s56, 7
	s_mul_i32 s56, s56, 128
	s_add_u32 s56, s56, s30
	buffer_load_dwordx4 v4, s[4:7], s56 offen lds
	buffer_load_dwordx4 v7, s[4:7], s56 offen offset:1024 lds
	s_add_u32 m0, s57, 114688
	s_add_u32 s56, s53, 3
	s_and_b32 s56, s56, 7
	s_mul_i32 s56, s56, 196608
	s_add_u32 s56, s56, s55
	s_add_u32 s56, s56, 1024
	buffer_load_dwordx4 v6, s[8:11], s56 offen lds
	buffer_load_dwordx4 v6, s[8:11], s56 offen offset:1024 lds
	s_add_u32 m0, s57, 122880
	s_add_u32 s56, s56, 98304
	buffer_load_dwordx4 v6, s[8:11], s56 offen lds
	buffer_load_dwordx4 v6, s[8:11], s56 offen offset:1024 lds
	s_waitcnt lgkmcnt(0)
	v_mfma_f32_16x16x32_bf16 v[100:103], v[128:131], v[128:131], 0
	v_mfma_f32_16x16x32_bf16 v[96:99], v[28:31], v[128:131], 0
	v_mfma_f32_16x16x32_bf16 v[100:103], v[132:135], v[132:135], v[100:103]
	v_mfma_f32_16x16x32_bf16 v[96:99], v[28:31], v[132:135], v[96:99]
	s_waitcnt vmcnt(12)
	v_readlane_b32 s60, v32, s52
	ds_read_b128 v[136:139], v22 offset:8192
	ds_read_b128 v[140:143], v25 offset:8192
	s_barrier
	s_add_u32 m0, s57, 32768
	s_add_u32 s56, s53, 4
	s_and_b32 s56, s56, 7
	s_mul_i32 s56, s56, 128
	s_add_u32 s56, s56, s30
	buffer_load_dwordx4 v4, s[4:7], s56 offen lds
	buffer_load_dwordx4 v7, s[4:7], s56 offen offset:1024 lds
	s_add_u32 m0, s57, 131072
	s_add_u32 s56, s53, 4
	s_and_b32 s56, s56, 7
	s_mul_i32 s56, s56, 196608
	s_add_u32 s56, s56, s55
	s_add_u32 s56, s56, 1024
	buffer_load_dwordx4 v6, s[8:11], s56 offen lds
	buffer_load_dwordx4 v6, s[8:11], s56 offen offset:1024 lds
	s_add_u32 m0, s57, 139264
	s_add_u32 s56, s56, 98304
	buffer_load_dwordx4 v6, s[8:11], s56 offen lds
	buffer_load_dwordx4 v6, s[8:11], s56 offen offset:1024 lds
	s_waitcnt lgkmcnt(0)
	v_mfma_f32_16x16x32_bf16 v[100:103], v[136:139], v[136:139], v[100:103]
	v_mfma_f32_16x16x32_bf16 v[96:99], v[28:31], v[136:139], v[96:99]
	v_mfma_f32_16x16x32_bf16 v[100:103], v[140:143], v[140:143], v[100:103]
	v_mfma_f32_16x16x32_bf16 v[96:99], v[28:31], v[140:143], v[96:99]
	s_waitcnt vmcnt(16)
	ds_read_b128 v[144:147], v22 offset:16384
	ds_read_b128 v[148:151], v25 offset:16384
	s_waitcnt vmcnt(12)
	s_barrier
	s_add_u32 m0, s57, 40960
	s_add_u32 s56, s53, 5
	s_and_b32 s56, s56, 7
	s_mul_i32 s56, s56, 128
	s_add_u32 s56, s56, s30
	buffer_load_dwordx4 v4, s[4:7], s56 offen lds
	buffer_load_dwordx4 v7, s[4:7], s56 offen offset:1024 lds
	s_add_u32 m0, s57, 147456
	s_add_u32 s56, s53, 5
	s_and_b32 s56, s56, 7
	s_mul_i32 s56, s56, 196608
	s_add_u32 s56, s56, s55
	s_add_u32 s56, s56, 1024
	buffer_load_dwordx4 v6, s[8:11], s56 offen lds
	buffer_load_dwordx4 v6, s[8:11], s56 offen offset:1024 lds
	s_add_u32 m0, s57, 155648
	s_add_u32 s56, s56, 98304
	buffer_load_dwordx4 v6, s[8:11], s56 offen lds
	buffer_load_dwordx4 v6, s[8:11], s56 offen offset:1024 lds
	s_waitcnt lgkmcnt(0)
	v_mfma_f32_16x16x32_bf16 v[100:103], v[144:147], v[144:147], v[100:103]
	v_mfma_f32_16x16x32_bf16 v[96:99], v[28:31], v[144:147], v[96:99]
	v_mfma_f32_16x16x32_bf16 v[100:103], v[148:151], v[148:151], v[100:103]
	v_mfma_f32_16x16x32_bf16 v[96:99], v[28:31], v[148:151], v[96:99]
	s_waitcnt vmcnt(16)
	ds_read_b128 v[152:155], v22 offset:24576
	ds_read_b128 v[156:159], v25 offset:24576
	s_waitcnt vmcnt(12)
	s_barrier
	s_add_u32 m0, s57, 49152
	s_add_u32 s56, s53, 6
	s_and_b32 s56, s56, 7
	s_mul_i32 s56, s56, 128
	s_add_u32 s56, s56, s30
	buffer_load_dwordx4 v4, s[4:7], s56 offen lds
	buffer_load_dwordx4 v7, s[4:7], s56 offen offset:1024 lds
	s_add_u32 m0, s57, 65536
	s_add_u32 s56, s53, 6
	s_and_b32 s56, s56, 7
	s_mul_i32 s56, s56, 196608
	s_add_u32 s56, s56, s55
	s_add_u32 s56, s56, 1024
	buffer_load_dwordx4 v6, s[8:11], s56 offen lds
	buffer_load_dwordx4 v6, s[8:11], s56 offen offset:1024 lds
	s_add_u32 m0, s57, 73728
	s_add_u32 s56, s56, 98304
	buffer_load_dwordx4 v6, s[8:11], s56 offen lds
	buffer_load_dwordx4 v6, s[8:11], s56 offen offset:1024 lds
	s_waitcnt lgkmcnt(0)
	v_mfma_f32_16x16x32_bf16 v[100:103], v[152:155], v[152:155], v[100:103]
	v_mfma_f32_16x16x32_bf16 v[96:99], v[28:31], v[152:155], v[96:99]
	v_mfma_f32_16x16x32_bf16 v[100:103], v[156:159], v[156:159], v[100:103]
	v_mfma_f32_16x16x32_bf16 v[96:99], v[28:31], v[156:159], v[96:99]
	s_waitcnt vmcnt(16)
	ds_read_b128 v[160:163], v22 offset:32768
	ds_read_b128 v[164:167], v25 offset:32768
	s_waitcnt vmcnt(12)
	s_barrier
	s_add_u32 m0, s57, 57344
	s_add_u32 s56, s53, 7
	s_and_b32 s56, s56, 7
	s_mul_i32 s56, s56, 128
	s_add_u32 s56, s56, s30
	buffer_load_dwordx4 v4, s[4:7], s56 offen lds
	buffer_load_dwordx4 v7, s[4:7], s56 offen offset:1024 lds
	s_add_u32 m0, s57, 81920
	s_add_u32 s56, s53, 7
	s_and_b32 s56, s56, 7
	s_mul_i32 s56, s56, 196608
	s_add_u32 s56, s56, s55
	s_add_u32 s56, s56, 1024
	buffer_load_dwordx4 v6, s[8:11], s56 offen lds
	buffer_load_dwordx4 v6, s[8:11], s56 offen offset:1024 lds
	s_add_u32 m0, s57, 90112
	s_add_u32 s56, s56, 98304
	buffer_load_dwordx4 v6, s[8:11], s56 offen lds
	buffer_load_dwordx4 v6, s[8:11], s56 offen offset:1024 lds
	s_add_u32 m0, s62, 8192
	s_nop 0
	buffer_load_dwordx4 v18, s[16:19], s35 offen lds
	s_add_u32 m0, s62, 12288
	s_add_u32 s35, s35, s63
	buffer_load_dwordx4 v18, s[16:19], s35 offen lds
	s_add_u32 m0, s62, 16384
	s_add_u32 s35, s35, s63
	buffer_load_dwordx4 v18, s[16:19], s35 offen lds
	s_add_u32 m0, s62, 20480
	s_add_u32 s35, s35, s63
	buffer_load_dwordx4 v18, s[16:19], s35 offen lds
	s_waitcnt lgkmcnt(0)
	v_mfma_f32_16x16x32_bf16 v[100:103], v[160:163], v[160:163], v[100:103]
	v_mfma_f32_16x16x32_bf16 v[96:99], v[28:31], v[160:163], v[96:99]
	v_mfma_f32_16x16x32_bf16 v[100:103], v[164:167], v[164:167], v[100:103]
	v_mfma_f32_16x16x32_bf16 v[96:99], v[28:31], v[164:167], v[96:99]
	s_waitcnt vmcnt(20)
	ds_read_b128 v[168:171], v22 offset:40960
	ds_read_b128 v[172:175], v25 offset:40960
	s_waitcnt vmcnt(16)
	s_barrier
	s_add_u32 m0, s62, 98304
	s_add_u32 s56, s53, 0
	s_and_b32 s56, s56, 7
	s_mul_i32 s56, s56, 196608
	s_add_u32 s56, s56, s55
	buffer_load_dwordx4 v6, s[8:11], s56 offen lds
	s_add_u32 m0, s62, 102400
	s_add_u32 s56, s56, 98304
	buffer_load_dwordx4 v6, s[8:11], s56 offen lds
	s_add_u32 m0, s62, 106496
	s_add_u32 s56, s53, 1
	s_and_b32 s56, s56, 7
	s_mul_i32 s56, s56, 196608
	s_add_u32 s56, s56, s55
	buffer_load_dwordx4 v6, s[8:11], s56 offen lds
	s_add_u32 m0, s62, 110592
	s_add_u32 s56, s56, 98304
	buffer_load_dwordx4 v6, s[8:11], s56 offen lds
	s_waitcnt lgkmcnt(0)
	v_mfma_f32_16x16x32_bf16 v[100:103], v[168:171], v[168:171], v[100:103]
	v_mfma_f32_16x16x32_bf16 v[96:99], v[28:31], v[168:171], v[96:99]
	v_mfma_f32_16x16x32_bf16 v[100:103], v[172:175], v[172:175], v[100:103]
	v_mfma_f32_16x16x32_bf16 v[96:99], v[28:31], v[172:175], v[96:99]
	s_waitcnt vmcnt(18)
	ds_read_b128 v[176:179], v22 offset:49152
	ds_read_b128 v[180:183], v25 offset:49152
	s_waitcnt vmcnt(14)
	s_barrier
	s_add_u32 m0, s62, 24576
	s_add_u32 s56, s53, 2
	s_and_b32 s56, s56, 7
	s_mul_i32 s56, s56, 196608
	s_add_u32 s56, s56, s55
	buffer_load_dwordx4 v6, s[8:11], s56 offen lds
	s_add_u32 m0, s62, 28672
	s_add_u32 s56, s56, 98304
	buffer_load_dwordx4 v6, s[8:11], s56 offen lds
	s_add_u32 m0, s62, 32768
	s_add_u32 s56, s53, 3
	s_and_b32 s56, s56, 7
	s_mul_i32 s56, s56, 196608
	s_add_u32 s56, s56, s55
	buffer_load_dwordx4 v6, s[8:11], s56 offen lds
	s_add_u32 m0, s62, 36864
	s_add_u32 s56, s56, 98304
	buffer_load_dwordx4 v6, s[8:11], s56 offen lds
	s_waitcnt lgkmcnt(0)
	v_mfma_f32_16x16x32_bf16 v[100:103], v[176:179], v[176:179], v[100:103]
	v_mfma_f32_16x16x32_bf16 v[96:99], v[28:31], v[176:179], v[96:99]
	v_mfma_f32_16x16x32_bf16 v[100:103], v[180:183], v[180:183], v[100:103]
	v_mfma_f32_16x16x32_bf16 v[96:99], v[28:31], v[180:183], v[96:99]
	s_waitcnt vmcnt(16)
	ds_read_b128 v[184:187], v22 offset:57344
	ds_read_b128 v[188:191], v25 offset:57344
	s_waitcnt vmcnt(12)
	s_barrier
	s_waitcnt lgkmcnt(0)
	v_mfma_f32_16x16x32_bf16 v[100:103], v[184:187], v[184:187], v[100:103]
	v_mfma_f32_16x16x32_bf16 v[96:99], v[28:31], v[184:187], v[96:99]
	v_mfma_f32_16x16x32_bf16 v[100:103], v[188:191], v[188:191], v[100:103]
	v_mfma_f32_16x16x32_bf16 v[96:99], v[28:31], v[188:191], v[96:99]
	s_add_u32 m0, s62, 114688
	s_add_u32 s56, s53, 4
	s_and_b32 s56, s56, 7
	s_mul_i32 s56, s56, 196608
	s_add_u32 s56, s56, s55
	buffer_load_dwordx4 v6, s[8:11], s56 offen lds
	s_mov_b32 vcc_lo, 0xaaaaaaaa
	s_mov_b32 vcc_hi, vcc_lo
	s_nop 2
	v_cndmask_b32 v100, v100, v101, vcc
	v_cndmask_b32 v102, v102, v103, vcc
	s_mov_b32 vcc_lo, 0xcccccccc
	s_mov_b32 vcc_hi, vcc_lo
	v_cndmask_b32 v100, v100, v102, vcc
	s_mov_b32 exec_lo, 0x00f0000f
	s_mov_b32 exec_hi, 0xf0000f00
	v_mul_f32 v96, 0xbb000000, v96
	v_mul_f32 v97, v96, v96
	v_fma_f32 v100, v100, s49, -v97
	v_cvt_pk_bf16_f32 v98, v96, v96
	v_max_f32 v100, 0, v100
	v_lshlrev_b32 v99, 16, v98
	v_add_f32 v100, 0x3727c5ac, v100
	v_sub_f32 v99, v96, v99
	v_rsq_f32 v100, v100
	v_cvt_pk_bf16_f32 v99, v99, 0
	ds_write_b64 v23, v[98:99]
	ds_write_b32 v24, v100 offset:512
	s_mov_b64 exec, -1
	s_add_u32 m0, s62, 118784
	s_add_u32 s56, s56, 98304
	buffer_load_dwordx4 v6, s[8:11], s56 offen lds
	s_add_u32 m0, s62, 122880
	s_add_u32 s56, s53, 5
	s_and_b32 s56, s56, 7
	s_mul_i32 s56, s56, 196608
	s_add_u32 s56, s56, s55
	buffer_load_dwordx4 v6, s[8:11], s56 offen lds
	s_add_u32 m0, s62, 126976
	s_add_u32 s56, s56, 98304
	buffer_load_dwordx4 v6, s[8:11], s56 offen lds
	s_waitcnt lgkmcnt(0)
	s_barrier
	s_add_u32 m0, s62, 131072
	s_add_u32 s56, s53, 6
	s_and_b32 s56, s56, 7
	s_mul_i32 s56, s56, 196608
	s_add_u32 s56, s56, s55
	buffer_load_dwordx4 v6, s[8:11], s56 offen lds
	s_add_u32 m0, s62, 135168
	s_add_u32 s56, s56, 98304
	buffer_load_dwordx4 v6, s[8:11], s56 offen lds
	s_add_u32 m0, s62, 139264
	s_add_u32 s56, s53, 7
	s_and_b32 s56, s56, 7
	s_mul_i32 s56, s56, 196608
	s_add_u32 s56, s56, s55
	buffer_load_dwordx4 v6, s[8:11], s56 offen lds
	s_add_u32 m0, s62, 143360
	s_add_u32 s56, s56, 98304
	buffer_load_dwordx4 v6, s[8:11], s56 offen lds
	s_mov_b32 s63, s31
	buffer_load_dword v33, v34, s[12:15], s63 offen sc1
	s_mov_b32 s16, s36
	s_and_b32 s17, s37, 0xffff
	s_mov_b32 s18, 0x80000
	v_lshrrev_b32 v18, 3, v126
	v_lshlrev_b32 v17, 4, v5
	v_lshl_or_b32 v123, v18, 10, v17
	s_lshl_b32 s40, s3, 6
	s_add_u32 s40, s40, s29
	s_lshl_b32 s40, s40, 7
	s_mov_b32 s43, s62
	v_mov_b32 v46, v4
	v_mov_b32 v47, v7
	s_lshl_b32 s26, s28, 16
	s_add_u32 s26, s26, s40
	s_add_u32 s27, s57, 155648
	v_and_b32 v14, 27, v0
	v_cmp_eq_u32 vcc, 16, v14
	v_mov_b32 v17, 0x3f803f80
	v_and_b32 v18, 15, v0
	v_cndmask_b32 v14, 0, v17, vcc
	v_lshlrev_b32 v18, 4, v18
	v_lshl_or_b32 v122, v8, 8, v18
	v_mov_b32 v120, 0xdddd
	s_and_b32 s50, s3, 3
	s_lshl_b32 s36, s29, 2
	s_add_u32 s36, s36, s50
	s_lshr_b32 s37, s3, 2
	s_lshl_b32 s38, s36, 17
	s_lshl_b32 s59, s37, 11
	s_add_u32 s38, s38, s59
	s_lshl_b32 s56, s52, 12
	s_and_b32 s57, s28, 24
	s_lshl_b32 s57, s57, 12
	s_add_u32 s57, s57, s38
	v_mov_b32 v76, v14
	v_mov_b32 v77, v14
	v_mov_b32 v78, v14
	v_mov_b32 v79, v14
	v_mov_b32 v80, v14
	v_mov_b32 v81, v14
	v_mov_b32 v82, v14
	v_mov_b32 v83, v14
	s_lshl_b32 s59, s37, 15
	s_add_u32 s59, s59, s54
	s_lshl_b32 s32, s36, 10
	s_add_u32 s32, s32, s59
	s_add_u32 s32, s32, 4194304
	v_mov_b32 v88, v14
	v_mov_b32 v89, v14
	v_mov_b32 v90, v14
	v_mov_b32 v91, v14
	v_mov_b32 v92, v14
	v_mov_b32 v93, v14
	v_mov_b32 v94, v14
	v_mov_b32 v95, v14
	v_mov_b32 v100, v14
	v_mov_b32 v101, v14
	v_mov_b32 v102, v14
	v_mov_b32 v103, v14
	v_mov_b32 v104, v14
	v_mov_b32 v105, v14
	v_mov_b32 v106, v14
	v_mov_b32 v107, v14
	v_mov_b32 v112, v14
	v_mov_b32 v113, v14
	v_mov_b32 v114, v14
	v_mov_b32 v115, v14
	v_mov_b32 v116, v14
	v_mov_b32 v117, v14
	v_mov_b32 v118, v14
	v_mov_b32 v119, v14
	s_cmp_eq_u32 s37, 0
	s_cselect_b32 s58, s58, 1879048192
	s_add_u32 s33, s32, 2097152
	s_lshl_b32 s59, s29, 3
	s_add_u32 s59, s59, s3
	s_waitcnt vmcnt(13)
	s_barrier
	s_add_u32 m0, s43, 61440
	s_add_u32 s44, s40, 491520
	buffer_load_dwordx4 v123, s[16:19], s44 offen lds
	s_lshl_b32 s39, s59, 15
	s_add_u32 s39, s39, 15728640
	s_cmp_eq_u32 s29, 7
	s_cselect_b32 s39, 1879048192, s39
	s_lshl_b32 s30, s28, 3
	s_add_u32 s30, s30, s29
	s_lshl_b32 s30, s30, 10
	s_add_u32 s30, s30, 19922944
	s_mul_i32 s35, s3, 7
	s_add_u32 m0, s43, 57344
	s_add_u32 s44, s40, 458752
	buffer_load_dwordx4 v123, s[16:19], s44 offen lds
	s_add_u32 s35, s35, s29
	s_lshl_b32 s35, s35, 11
	s_mul_i32 s31, s28, 229376
	s_add_u32 s31, s31, 8388608
	s_add_u32 s35, s35, s31
	s_cmp_eq_u32 s29, 7
	s_cselect_b32 s35, 1879048192, s35
	s_add_u32 s45, s35, 114688
	s_cmp_eq_u32 s3, 0
	s_cselect_b32 s34, s30, 1879048192
	s_add_u32 m0, s43, 53248
	s_add_u32 s44, s40, 425984
	buffer_load_dwordx4 v123, s[16:19], s44 offen lds
	s_mul_i32 s59, s29, 229376
	s_add_u32 s59, s59, 17825792
	s_mul_i32 s41, s3, 28672
	s_add_u32 s41, s41, s59
	s_and_b32 s53, s28, 7
	s_lshl_b32 s53, s53, 3
	s_add_u32 s53, s53, s3
	s_mul_i32 s53, s53, 3584
	s_add_u32 s53, s53, s59
	s_waitcnt vmcnt(8)
	s_barrier
	s_add_u32 m0, s43, 49152
	s_add_u32 s44, s40, 393216
	buffer_load_dwordx4 v123, s[16:19], s44 offen lds
	s_lshl_b32 s54, s28, 16
	s_lshl_b32 s42, s29, 7
	s_add_u32 s42, s42, s54
	s_lshl_b32 s51, s28, 7
	s_add_u32 s51, s51, 20217856
	s_cmp_eq_u32 s37, 0
	s_cselect_b32 s48, s29, s29
	s_cselect_b32 s59, s28, s28
	s_lshl_b32 s48, s48, 5
	s_add_u32 s48, s48, s59
	s_add_u32 m0, s43, 45056
	s_add_u32 s44, s40, 360448
	buffer_load_dwordx4 v123, s[16:19], s44 offen lds
	s_lshl_b32 s48, s48, 5
	s_add_u32 s48, s48, s3
	s_lshl_b32 s48, s48, 2
	s_add_u32 s48, s48, 20217856
	v_lshrrev_b32 v18, 3, v126
	v_lshlrev_b32 v17, 2, v3
	v_lshl_or_b32 v124, v18, 12, v17
	v_min_u32 v125, 27, v126
	s_add_u32 m0, s43, 40960
	s_add_u32 s44, s40, 327680
	buffer_load_dwordx4 v123, s[16:19], s44 offen lds
	v_lshlrev_b32 v125, 7, v125
	s_lshl_b32 s59, s50, 11
	s_lshl_b32 s62, s37, 10
	s_add_u32 s59, s59, s62
	s_add_u32 s59, s59, 81920
	v_add_u32 v64, s59, v121
	v_xor_b32 v68, 0x400, v64
	s_mul_i32 s62, s3, 2
	s_lshr_b32 s62, s62, 3
	s_waitcnt vmcnt(6)
	ds_read_b64_tr_b16 v[128:129], v44
	ds_read_b64_tr_b16 v[130:131], v44 offset:4096
	ds_read_b64_tr_b16 v[132:133], v44 offset:8192
	ds_read_b64_tr_b16 v[134:135], v44 offset:12288
	ds_read_b64_tr_b16 v[136:137], v45
	ds_read_b64_tr_b16 v[138:139], v45 offset:4096
	ds_read_b64_tr_b16 v[140:141], v45 offset:8192
	ds_read_b64_tr_b16 v[142:143], v45 offset:12288
	s_waitcnt lgkmcnt(0)
	s_add_u32 m0, s43, 20480
	s_add_u32 s44, s40, 163840
	buffer_load_dwordx4 v123, s[16:19], s44 offen lds
	s_add_u32 m0, s43, 16384
	s_add_u32 s44, s40, 131072
	buffer_load_dwordx4 v123, s[16:19], s44 offen lds
	s_add_u32 m0, s43, 12288
	s_add_u32 s44, s40, 98304
	buffer_load_dwordx4 v123, s[16:19], s44 offen lds
	s_add_u32 m0, s43, 8192
	s_add_u32 s44, s40, 65536
	buffer_load_dwordx4 v123, s[16:19], s44 offen lds
	s_cmp_lg_u32 s3, 3
	s_cbranch_scc1 .Lk_n012
	v_and_b32 v35, 16, v121
	v_add_u32 v35, 90112, v35
	s_add_u32 s60, s60, 64
	v_cmp_eq_u32 vcc, v32, v33
	s_cbranch_vccnz .Lk_gw
.Lk_n012:
	s_barrier
	s_cmp_lg_u32 s3, 3
	s_cbranch_scc1 .Lk_n013
	ds_read_b128 v[36:39], v35
	s_lshl_b32 s63, s29, 5
	s_add_u32 s63, s63, s28
	s_lshl_b32 s63, s63, 7
	s_add_u32 s63, s63, 20185088
	s_mov_b64 exec, 3
	s_waitcnt lgkmcnt(0)
	buffer_store_dwordx4 v[36:39], v121, s[12:15], s63 offen sc1
	s_mov_b64 exec, -1
.Lk_n013:
	s_add_u32 s44, s56, s57
	v_and_b32 v127, 0x330, v121
	v_and_b32 v72, 64, v121
	v_lshl_or_b32 v127, v72, 1, v127
	v_lshrrev_b32 v72, 1, v121
	v_and_or_b32 v127, v72, 64, v127
	buffer_load_dwordx4 v[72:75], v127, s[12:15], s44 offen sc1
	s_mov_b64 exec, s[46:47]
	buffer_load_dwordx4 v[76:79], v122, s[12:15], s44 offen offset:1024 sc1
	buffer_load_dwordx4 v[80:83], v122, s[12:15], s44 offen offset:1536 sc1
	s_mov_b64 exec, -1
	s_add_u32 m0, s43, 4096
	s_add_u32 s44, s40, 32768
	buffer_load_dwordx4 v123, s[16:19], s44 offen lds
	v_mov_b32 v0, 0
	v_mov_b32 v1, 0
	v_mov_b32 v2, 0
	v_mov_b32 v3, 0
	v_mov_b32 v4, 0
	s_add_u32 m0, s43, 0
	s_add_u32 s44, s40, 0
	buffer_load_dwordx4 v123, s[16:19], s44 offen lds
	v_mov_b32 v5, 0
	v_mov_b32 v7, 0
	v_mov_b32 v8, 0
	v_mov_b32 v9, 0
	v_mov_b32 v14, 0
	s_mov_b32 m0, s27
	s_nop 0
	buffer_load_dwordx4 v46, s[4:7], s26 offen lds
	buffer_load_dwordx4 v47, s[4:7], s26 offen offset:1024 lds
	v_mov_b32 v17, 0
	v_mov_b32 v18, 0
	v_mov_b32 v19, 0
	v_mov_b32 v21, 0
	v_mov_b32 v22, 0
	s_add_u32 m0, s43, 36864
	s_add_u32 s44, s40, 294912
	buffer_load_dwordx4 v123, s[16:19], s44 offen lds
	v_mov_b32 v23, 0
	v_mov_b32 v252, -1
	v_mov_b32 v253, -1
	v_mov_b32 v254, -1
	v_mov_b32 v255, -1
	s_add_u32 m0, s43, 32768
	s_add_u32 s44, s40, 262144
	buffer_load_dwordx4 v123, s[16:19], s44 offen lds
	v_mov_b32 v15, 0
	v_mov_b32 v16, 0
	v_mov_b32 v28, 0
	v_mov_b32 v29, 0
	v_mov_b32 v30, 0
	s_add_u32 m0, s43, 28672
	s_add_u32 s44, s40, 229376
	buffer_load_dwordx4 v123, s[16:19], s44 offen lds
	v_mov_b32 v31, 0
	v_mov_b32 v10, 0
	v_mov_b32 v11, 0
	v_mov_b32 v12, 0
	v_mov_b32 v13, 0
	s_add_u32 m0, s43, 24576
	s_add_u32 s44, s40, 196608
	buffer_load_dwordx4 v123, s[16:19], s44 offen lds
	v_mov_b32 v24, 0
	v_mov_b32 v25, 0
	v_mov_b32 v26, 0
	v_mov_b32 v27, 0
	v_mov_b32 v6, 0
	v_mov_b32 v20, 0
	s_cmp_lg_u32 s3, 3
	s_cbranch_scc1 .Lk_pt
	s_and_b32 s63, s63, 0xfffffc00
.Lk_kp:
	buffer_load_dword v33, v34, s[12:15], s63 offen sc1
	s_waitcnt vmcnt(0)
	v_cmp_eq_u32 vcc, 0, v33
	s_cbranch_vccz .Lk_pt3
	s_sub_u32 s61, s61, 1
	s_cbranch_scc1 .Lk_end
	s_add_u32 s60, s60, 1
	v_mov_b32 v35, s60
	buffer_store_dword v35, off, s[12:15], s58 sc1
	s_branch .Lk_kp
.Lk_pt:
	s_sleep 12
.Lk_pt3:
	buffer_store_dwordx4 v[128:131], v121, s[12:15], s24 offen sc1
	buffer_store_dwordx4 v[132:135], v121, s[12:15], s24 offen offset:1024 sc1
	buffer_store_dwordx4 v[136:139], v121, s[12:15], s25 offen sc1
	buffer_store_dwordx4 v[140:143], v121, s[12:15], s25 offen offset:1024 sc1
	s_branch .Lk_b12
.Lk_gw:
	s_sub_u32 s61, s61, 1
	s_cbranch_scc1 .Lk_end
	buffer_load_dword v33, v34, s[12:15], s63 offen sc1
	s_waitcnt vmcnt(0)
	v_cmp_eq_u32 vcc, v32, v33
	s_cbranch_vccz .Lk_n012
	s_add_u32 s60, s60, 1
	v_mov_b32 v36, s60
	buffer_store_dword v36, off, s[12:15], s58 sc1
	s_sleep 4
	s_branch .Lk_gw
.Lk_G:
	s_add_u32 m0, s57, 65536
	s_add_u32 s56, s53, 0
	s_and_b32 s56, s56, 7
	s_mul_i32 s56, s56, 196608
	s_add_u32 s56, s56, s55
	s_add_u32 s56, s56, 1024
	buffer_load_dwordx4 v6, s[8:11], s56 offen lds
	buffer_load_dwordx4 v6, s[8:11], s56 offen offset:1024 lds
	s_add_u32 m0, s57, 73728
	s_add_u32 s56, s56, 98304
	buffer_load_dwordx4 v6, s[8:11], s56 offen lds
	buffer_load_dwordx4 v6, s[8:11], s56 offen offset:1024 lds
	s_add_u32 m0, s57, 81920
	s_add_u32 s56, s53, 1
	s_and_b32 s56, s56, 7
	s_mul_i32 s56, s56, 196608
	s_add_u32 s56, s56, s55
	s_add_u32 s56, s56, 1024
	buffer_load_dwordx4 v6, s[8:11], s56 offen lds
	buffer_load_dwordx4 v6, s[8:11], s56 offen offset:1024 lds
	s_add_u32 m0, s57, 90112
	s_add_u32 s56, s56, 98304
	buffer_load_dwordx4 v6, s[8:11], s56 offen lds
	buffer_load_dwordx4 v6, s[8:11], s56 offen offset:1024 lds
	s_mov_b32 s31, 20250624
	s_cmp_eq_u32 s50, 3
	s_cselect_b32 s31, s31, 1879048192
	s_lshl_b32 s58, s52, 7
	s_add_u32 s58, s58, s31
	s_and_b32 s59, s2, 0xc7
	s_cmp_eq_u32 s59, 2
	s_cselect_b32 s58, s58, 1879048192
	s_min_u32 s58, s58, 1879048192
	v_bfe_u32 v21, v0, 1, 1
	v_bfe_u32 v22, v0, 4, 1
	v_lshl_or_b32 v21, v22, 1, v21
	v_bfe_u32 v22, v0, 3, 1
	v_lshl_or_b32 v21, v22, 2, v21
	v_lshlrev_b32 v21, 4, v21
	v_and_b32 v22, 1, v0
	v_lshl_or_b32 v21, v22, 3, v21
	v_bfe_u32 v22, v0, 2, 2
	v_lshl_or_b32 v21, v22, 7, v21
	v_and_b32 v9, 31, v0
	v_bfe_u32 v24, v0, 4, 1
	s_lshr_b32 s51, s50, 1
	s_and_b32 s50, s50, 1
	s_lshl_b32 s59, s50, 5
	v_bfe_u32 v22, v0, 1, 3
	v_xor_b32 v22, v22, v8
	v_or_b32 v9, s59, v9
	v_lshlrev_b32 v4, 3, v9
	v_lshlrev_b32 v7, 2, v9
	v_lshlrev_b32 v9, 7, v9
	v_lshl_or_b32 v9, v22, 4, v9
	v_xor_b32 v9, s59, v9
	s_lshl_b32 s59, s50, 7
	v_lshl_add_u32 v1, v8, 4, s59
	v_lshrrev_b32 v28, 2, v0
	v_xor_b32 v28, v28, v0
	v_and_b32 v28, 4, v28
	v_cmp_eq_u32 vcc, 0, v28
	v_mov_b32 v31, 0x3f003f00
	s_nop 0
	v_cndmask_b32 v28, 0, v31, vcc
	v_cndmask_b32 v29, 0, v31, vcc
	v_cndmask_b32 v30, 0, v31, vcc
	v_cndmask_b32 v31, 0, v31, vcc
	v_xor_b32 v10, 32, v9
	v_xor_b32 v11, 64, v9
	v_xor_b32 v12, 0x60, v9
	s_lshl_b32 s59, s51, 6
	v_xor_b32 v22, s59, v21
	v_lshl_or_b32 v23, v8, 11, v22
	s_lshl_b32 s59, s50, 12
	s_add_u32 s59, s59, 0x10000
	v_or_b32 v13, s59, v23
	v_xor_b32 v14, 0x1000, v13
	v_lshl_or_b32 v23, v8, 10, v22
	s_lshl_b32 s59, s50, 11
	s_add_u32 s59, s59, 98304
	v_or_b32 v15, s59, v23
	v_xor_b32 v16, 0x800, v15
	v_lshlrev_b32 v21, 2, v126
	v_lshl_add_u32 v21, s3, 9, v21
	v_and_b32 v22, 0x33, v126
	v_and_b32 v23, 4, v0
	v_lshl_or_b32 v22, v23, 1, v22
	v_and_b32 v23, 8, v0
	v_lshrrev_b32 v23, 1, v23
	v_or_b32 v22, v22, v23
	v_mov_b32 v17, v121
	v_and_b32 v22, 15, v0
	v_lshlrev_b32 v22, 4, v22
	v_lshl_or_b32 v22, v8, 8, v22
	v_lshl_or_b32 v18, v24, 17, v22
	s_lshl_b32 s59, s50, 4
	s_lshl_b32 s63, s51, 3
	s_add_u32 s59, s59, s63
	s_add_u32 s59, s59, 90112
	v_lshl_add_u32 v19, v8, 2, s59
	s_lshl_b32 s59, s29, 2
	s_lshl_b32 s63, s51, 1
	s_add_u32 s59, s59, s63
	s_lshl_b32 s59, s59, 6
	s_lshl_b32 s63, s28, 1
	s_add_u32 s59, s59, s63
	s_add_u32 s59, s59, s50
	s_lshl_b32 s32, s59, 11
	s_add_u32 s33, s32, 0x20000
	s_lshl_b32 s59, s51, 12
	s_lshl_b32 s63, s50, 10
	s_add_u32 s59, s59, s63
	s_add_u32 s59, s59, 81920
	v_add_u32 v20, s59, v121
	v_mov_b32 v26, 0
	v_mov_b32 v27, 0
	s_setprio 2
	s_cmp_lg_u32 s3, 6
	s_cbranch_scc1 .Lk_t1
	s_mov_b32 s16, s36
	s_and_b32 s17, s37, 0xffff
	s_mov_b32 s18, 0x80000
	v_lshlrev_b32 v24, 10, v126
	s_lshl_b32 s59, s28, 14
	s_add_u32 s59, s59, s55
	s_mov_b64 exec, 0xffff
	buffer_load_dword v127, v24, s[16:19], s59 offen
	s_mov_b64 exec, -1
	s_branch .Lk_pulled
.Lk_t1:
	buffer_load_dword v127, off, s[8:11], s10
.Lk_pulled:
	s_cmp_lg_u32 s58, 1879048192
	s_cbranch_scc1 .Lk_zero
	s_waitcnt vmcnt(5)
.Lk_zret:
	s_barrier
	s_cmp_ge_u32 s29, 3
	s_cbranch_scc1 .Lk_nokw
	s_lshl_b32 s26, s48, 12
	s_add_u32 s26, s26, s62
	v_mul_u32_u24 v25, 98304, v126
	s_mov_b64 exec, 0xffff
	buffer_load_dword v127, v25, s[8:11], s26 offen offset:384
	s_mov_b64 exec, -1
.Lk_nokw:
	ds_read_b128 v[128:131], v9
	ds_read_b64_tr_b16 v[32:33], v13
	ds_read_b64_tr_b16 v[34:35], v13 offset:512
	ds_read_b64_tr_b16 v[36:37], v13 offset:1024
	ds_read_b64_tr_b16 v[38:39], v13 offset:1536
	ds_read_b128 v[132:135], v10
	ds_read_b64_tr_b16 v[40:41], v14
	ds_read_b64_tr_b16 v[42:43], v14 offset:512
	ds_read_b64_tr_b16 v[44:45], v14 offset:1024
	ds_read_b64_tr_b16 v[46:47], v14 offset:1536
	ds_read_b128 v[136:139], v11
	ds_read_b64_tr_b16 v[48:49], v13 offset:8192
	ds_read_b64_tr_b16 v[50:51], v13 offset:8704
	ds_read_b64_tr_b16 v[52:53], v13 offset:9216
	ds_read_b64_tr_b16 v[54:55], v13 offset:9728
	s_waitcnt lgkmcnt(10)
	v_mfma_f32_32x32x16_bf16 v[64:79], v[32:35], v[128:131], 0
	ds_read_b128 v[140:143], v12
	ds_read_b64_tr_b16 v[56:57], v14 offset:8192
	v_mfma_f32_32x32x16_bf16 v[80:95], v[128:131], v[36:39], 0
	ds_read_b64_tr_b16 v[58:59], v14 offset:8704
	ds_read_b64_tr_b16 v[60:61], v14 offset:9216
	v_mfma_f32_16x16x32_bf16 v[96:99], v[28:31], v[32:35], 0
	ds_read_b64_tr_b16 v[62:63], v14 offset:9728
	v_mfma_f32_16x16x32_bf16 v[100:103], v[28:31], v[36:39], 0
	s_cmp_lg_u32 s58, 1879048192
	s_cbranch_scc1 .Lk_z1
	s_waitcnt vmcnt(1)
.Lk_z1:
	s_waitcnt vmcnt(3)
	s_barrier
	s_waitcnt lgkmcnt(10)
	v_mfma_f32_32x32x16_bf16 v[64:79], v[40:43], v[132:135], v[64:79]
	ds_read_b128 v[144:147], v9 offset:8192
	ds_read_b64_tr_b16 v[32:33], v13 offset:16384
	ds_read_b64_tr_b16 v[34:35], v13 offset:16896
	v_mfma_f32_32x32x16_bf16 v[80:95], v[132:135], v[44:47], v[80:95]
	ds_read_b64_tr_b16 v[36:37], v13 offset:17408
	ds_read_b64_tr_b16 v[38:39], v13 offset:17920
	s_waitcnt lgkmcnt(10)
	v_mfma_f32_32x32x16_bf16 v[64:79], v[48:51], v[136:139], v[64:79]
	ds_read_b128 v[148:151], v10 offset:8192
	ds_read_b64_tr_b16 v[40:41], v14 offset:16384
	v_mfma_f32_32x32x16_bf16 v[80:95], v[136:139], v[52:55], v[80:95]
	ds_read_b64_tr_b16 v[42:43], v14 offset:16896
	ds_read_b64_tr_b16 v[44:45], v14 offset:17408
	v_mfma_f32_16x16x32_bf16 v[96:99], v[28:31], v[48:51], v[96:99]
	ds_read_b64_tr_b16 v[46:47], v14 offset:17920
	v_mfma_f32_16x16x32_bf16 v[100:103], v[28:31], v[52:55], v[100:103]
	s_waitcnt lgkmcnt(10)
	v_mfma_f32_32x32x16_bf16 v[64:79], v[56:59], v[140:143], v[64:79]
	ds_read_b128 v[152:155], v11 offset:8192
	ds_read_b64_tr_b16 v[48:49], v13 offset:24576
	ds_read_b64_tr_b16 v[50:51], v13 offset:25088
	v_mfma_f32_32x32x16_bf16 v[80:95], v[140:143], v[60:63], v[80:95]
	ds_read_b64_tr_b16 v[52:53], v13 offset:25600
	ds_read_b64_tr_b16 v[54:55], v13 offset:26112
	s_waitcnt lgkmcnt(10)
	v_mfma_f32_32x32x16_bf16 v[64:79], v[32:35], v[144:147], v[64:79]
	ds_read_b128 v[156:159], v12 offset:8192
	ds_read_b64_tr_b16 v[56:57], v14 offset:24576
	v_mfma_f32_32x32x16_bf16 v[80:95], v[144:147], v[36:39], v[80:95]
	ds_read_b64_tr_b16 v[58:59], v14 offset:25088
	ds_read_b64_tr_b16 v[60:61], v14 offset:25600
	v_mfma_f32_16x16x32_bf16 v[96:99], v[28:31], v[32:35], v[96:99]
	ds_read_b64_tr_b16 v[62:63], v14 offset:26112
	v_mfma_f32_16x16x32_bf16 v[100:103], v[28:31], v[36:39], v[100:103]
	s_barrier
	s_cmp_eq_u32 s58, 1879048192
	s_cbranch_scc1 .Lk_b2
	s_waitcnt vmcnt(0)
	v_add_u32 v119, 1, v119
	buffer_store_dword v119, off, s[12:15], s58 sc1
.Lk_b2:
	s_waitcnt lgkmcnt(10)
	v_mfma_f32_32x32x16_bf16 v[64:79], v[40:43], v[148:151], v[64:79]
	ds_read_b128 v[160:163], v9 offset:16384
	ds_read_b64_tr_b16 v[32:33], v13 offset:32768
	ds_read_b64_tr_b16 v[34:35], v13 offset:33280
	v_mfma_f32_32x32x16_bf16 v[80:95], v[148:151], v[44:47], v[80:95]
	ds_read_b64_tr_b16 v[36:37], v13 offset:33792
	ds_read_b64_tr_b16 v[38:39], v13 offset:34304
	s_waitcnt lgkmcnt(10)
	v_mfma_f32_32x32x16_bf16 v[64:79], v[48:51], v[152:155], v[64:79]
	ds_read_b128 v[164:167], v10 offset:16384
	ds_read_b64_tr_b16 v[40:41], v14 offset:32768
	v_mfma_f32_32x32x16_bf16 v[80:95], v[152:155], v[52:55], v[80:95]
	ds_read_b64_tr_b16 v[42:43], v14 offset:33280
	ds_read_b64_tr_b16 v[44:45], v14 offset:33792
	v_mfma_f32_16x16x32_bf16 v[96:99], v[28:31], v[48:51], v[96:99]
	ds_read_b64_tr_b16 v[46:47], v14 offset:34304
	v_mfma_f32_16x16x32_bf16 v[100:103], v[28:31], v[52:55], v[100:103]
	s_waitcnt lgkmcnt(10)
	v_mfma_f32_32x32x16_bf16 v[64:79], v[56:59], v[156:159], v[64:79]
	ds_read_b128 v[168:171], v11 offset:16384
	ds_read_b64_tr_b16 v[48:49], v13 offset:40960
	ds_read_b64_tr_b16 v[50:51], v13 offset:41472
	v_mfma_f32_32x32x16_bf16 v[80:95], v[156:159], v[60:63], v[80:95]
	ds_read_b64_tr_b16 v[52:53], v13 offset:41984
	ds_read_b64_tr_b16 v[54:55], v13 offset:42496
	s_waitcnt lgkmcnt(10)
	v_mfma_f32_32x32x16_bf16 v[64:79], v[32:35], v[160:163], v[64:79]
	ds_read_b128 v[172:175], v12 offset:16384
	ds_read_b64_tr_b16 v[56:57], v14 offset:40960
	v_mfma_f32_32x32x16_bf16 v[80:95], v[160:163], v[36:39], v[80:95]
	ds_read_b64_tr_b16 v[58:59], v14 offset:41472
	ds_read_b64_tr_b16 v[60:61], v14 offset:41984
	v_mfma_f32_16x16x32_bf16 v[96:99], v[28:31], v[32:35], v[96:99]
	ds_read_b64_tr_b16 v[62:63], v14 offset:42496
	v_mfma_f32_16x16x32_bf16 v[100:103], v[28:31], v[36:39], v[100:103]
	s_barrier
	s_waitcnt lgkmcnt(10)
	v_mfma_f32_32x32x16_bf16 v[64:79], v[40:43], v[164:167], v[64:79]
	ds_read_b128 v[176:179], v9 offset:24576
	ds_read_b64_tr_b16 v[32:33], v13 offset:49152
	ds_read_b64_tr_b16 v[34:35], v13 offset:49664
	v_mfma_f32_32x32x16_bf16 v[80:95], v[164:167], v[44:47], v[80:95]
	ds_read_b64_tr_b16 v[36:37], v13 offset:50176
	ds_read_b64_tr_b16 v[38:39], v13 offset:50688
	s_waitcnt lgkmcnt(10)
	v_mfma_f32_32x32x16_bf16 v[64:79], v[48:51], v[168:171], v[64:79]
	ds_read_b128 v[180:183], v10 offset:24576
	ds_read_b64_tr_b16 v[40:41], v14 offset:49152
	v_mfma_f32_32x32x16_bf16 v[80:95], v[168:171], v[52:55], v[80:95]
	ds_read_b64_tr_b16 v[42:43], v14 offset:49664
	ds_read_b64_tr_b16 v[44:45], v14 offset:50176
	v_mfma_f32_16x16x32_bf16 v[96:99], v[28:31], v[48:51], v[96:99]
	ds_read_b64_tr_b16 v[46:47], v14 offset:50688
	v_mfma_f32_16x16x32_bf16 v[100:103], v[28:31], v[52:55], v[100:103]
	s_waitcnt lgkmcnt(10)
	v_mfma_f32_32x32x16_bf16 v[64:79], v[56:59], v[172:175], v[64:79]
	ds_read_b128 v[184:187], v11 offset:24576
	ds_read_b64_tr_b16 v[48:49], v13 offset:57344
	ds_read_b64_tr_b16 v[50:51], v13 offset:57856
	v_mfma_f32_32x32x16_bf16 v[80:95], v[172:175], v[60:63], v[80:95]
	ds_read_b64_tr_b16 v[52:53], v13 offset:58368
	ds_read_b64_tr_b16 v[54:55], v13 offset:58880
	s_waitcnt lgkmcnt(10)
	v_mfma_f32_32x32x16_bf16 v[64:79], v[32:35], v[176:179], v[64:79]
	ds_read_b128 v[188:191], v12 offset:24576
	ds_read_b64_tr_b16 v[56:57], v14 offset:57344
	v_mfma_f32_32x32x16_bf16 v[80:95], v[176:179], v[36:39], v[80:95]
	ds_read_b64_tr_b16 v[58:59], v14 offset:57856
	ds_read_b64_tr_b16 v[60:61], v14 offset:58368
	v_mfma_f32_16x16x32_bf16 v[96:99], v[28:31], v[32:35], v[96:99]
	ds_read_b64_tr_b16 v[62:63], v14 offset:58880
	v_mfma_f32_16x16x32_bf16 v[100:103], v[28:31], v[36:39], v[100:103]
	v_xor_b32 v13, 0x30000, v13
	v_xor_b32 v14, 0x30000, v14
	s_barrier
	s_cmp_ge_u32 s29, 3
	s_cbranch_scc1 .Lk_noww
	s_mov_b32 s16, s36
	s_and_b32 s17, s37, 0xffff
	s_mov_b32 s18, 0x80000
	s_mov_b64 exec, 63
	buffer_load_dword v127, v25, s[16:19], s26 offen offset:384
	s_mov_b64 exec, -1
.Lk_noww:
	s_waitcnt lgkmcnt(10)
	v_mfma_f32_32x32x16_bf16 v[64:79], v[40:43], v[180:183], v[64:79]
	ds_read_b128 v[192:195], v9 offset:32768
	ds_read_b64_tr_b16 v[32:33], v13
	ds_read_b64_tr_b16 v[34:35], v13 offset:512
	v_mfma_f32_32x32x16_bf16 v[80:95], v[180:183], v[44:47], v[80:95]
	ds_read_b64_tr_b16 v[36:37], v13 offset:1024
	ds_read_b64_tr_b16 v[38:39], v13 offset:1536
	s_waitcnt lgkmcnt(10)
	v_mfma_f32_32x32x16_bf16 v[64:79], v[48:51], v[184:187], v[64:79]
	ds_read_b128 v[196:199], v10 offset:32768
	ds_read_b64_tr_b16 v[40:41], v14
	v_mfma_f32_32x32x16_bf16 v[80:95], v[184:187], v[52:55], v[80:95]
	ds_read_b64_tr_b16 v[42:43], v14 offset:512
	ds_read_b64_tr_b16 v[44:45], v14 offset:1024
	v_mfma_f32_16x16x32_bf16 v[96:99], v[28:31], v[48:51], v[96:99]
	ds_read_b64_tr_b16 v[46:47], v14 offset:1536
	v_mfma_f32_16x16x32_bf16 v[100:103], v[28:31], v[52:55], v[100:103]
	s_waitcnt lgkmcnt(10)
	v_mfma_f32_32x32x16_bf16 v[64:79], v[56:59], v[188:191], v[64:79]
	ds_read_b128 v[200:203], v11 offset:32768
	ds_read_b64_tr_b16 v[48:49], v13 offset:8192
	ds_read_b64_tr_b16 v[50:51], v13 offset:8704
	v_mfma_f32_32x32x16_bf16 v[80:95], v[188:191], v[60:63], v[80:95]
	ds_read_b64_tr_b16 v[52:53], v13 offset:9216
	ds_read_b64_tr_b16 v[54:55], v13 offset:9728
	s_waitcnt lgkmcnt(10)
	v_mfma_f32_32x32x16_bf16 v[64:79], v[32:35], v[192:195], v[64:79]
	ds_read_b128 v[204:207], v12 offset:32768
	ds_read_b64_tr_b16 v[56:57], v14 offset:8192
	v_mfma_f32_32x32x16_bf16 v[80:95], v[192:195], v[36:39], v[80:95]
	ds_read_b64_tr_b16 v[58:59], v14 offset:8704
	ds_read_b64_tr_b16 v[60:61], v14 offset:9216
	v_mfma_f32_16x16x32_bf16 v[96:99], v[28:31], v[32:35], v[96:99]
	ds_read_b64_tr_b16 v[62:63], v14 offset:9728
	v_mfma_f32_16x16x32_bf16 v[100:103], v[28:31], v[36:39], v[100:103]
	s_barrier
	s_waitcnt lgkmcnt(10)
	v_mfma_f32_32x32x16_bf16 v[64:79], v[40:43], v[196:199], v[64:79]
	ds_read_b128 v[208:211], v9 offset:40960
	ds_read_b64_tr_b16 v[32:33], v13 offset:16384
	ds_read_b64_tr_b16 v[34:35], v13 offset:16896
	v_mfma_f32_32x32x16_bf16 v[80:95], v[196:199], v[44:47], v[80:95]
	ds_read_b64_tr_b16 v[36:37], v13 offset:17408
	ds_read_b64_tr_b16 v[38:39], v13 offset:17920
	s_waitcnt lgkmcnt(10)
	v_mfma_f32_32x32x16_bf16 v[64:79], v[48:51], v[200:203], v[64:79]
	ds_read_b128 v[212:215], v10 offset:40960
	ds_read_b64_tr_b16 v[40:41], v14 offset:16384
	v_mfma_f32_32x32x16_bf16 v[80:95], v[200:203], v[52:55], v[80:95]
	ds_read_b64_tr_b16 v[42:43], v14 offset:16896
	ds_read_b64_tr_b16 v[44:45], v14 offset:17408
	v_mfma_f32_16x16x32_bf16 v[96:99], v[28:31], v[48:51], v[96:99]
	ds_read_b64_tr_b16 v[46:47], v14 offset:17920
	v_mfma_f32_16x16x32_bf16 v[100:103], v[28:31], v[52:55], v[100:103]
	s_waitcnt lgkmcnt(10)
	v_mfma_f32_32x32x16_bf16 v[64:79], v[56:59], v[204:207], v[64:79]
	ds_read_b128 v[216:219], v11 offset:40960
	ds_read_b64_tr_b16 v[48:49], v13 offset:24576
	ds_read_b64_tr_b16 v[50:51], v13 offset:25088
	v_mfma_f32_32x32x16_bf16 v[80:95], v[204:207], v[60:63], v[80:95]
	ds_read_b64_tr_b16 v[52:53], v13 offset:25600
	ds_read_b64_tr_b16 v[54:55], v13 offset:26112
	s_waitcnt lgkmcnt(10)
	v_mfma_f32_32x32x16_bf16 v[64:79], v[32:35], v[208:211], v[64:79]
	ds_read_b128 v[220:223], v12 offset:40960
	ds_read_b64_tr_b16 v[56:57], v14 offset:24576
	v_mfma_f32_32x32x16_bf16 v[80:95], v[208:211], v[36:39], v[80:95]
	ds_read_b64_tr_b16 v[58:59], v14 offset:25088
	ds_read_b64_tr_b16 v[60:61], v14 offset:25600
	v_mfma_f32_16x16x32_bf16 v[96:99], v[28:31], v[32:35], v[96:99]
	ds_read_b64_tr_b16 v[62:63], v14 offset:26112
	v_mfma_f32_16x16x32_bf16 v[100:103], v[28:31], v[36:39], v[100:103]
	v_xor_b32 v13, 0x30000, v13
	v_xor_b32 v14, 0x30000, v14
	s_barrier
	s_cmp_eq_u32 s58, 1879048192
	s_cbranch_scc1 .Lk_b3
	s_waitcnt vmcnt(0)
	v_add_u32 v119, 1, v119
	buffer_store_dword v119, off, s[12:15], s58 sc1
.Lk_b3:
	s_waitcnt lgkmcnt(10)
	v_mfma_f32_32x32x16_bf16 v[64:79], v[40:43], v[212:215], v[64:79]
	ds_read_b128 v[224:227], v9 offset:49152
	ds_read_b64_tr_b16 v[32:33], v13
	ds_read_b64_tr_b16 v[34:35], v13 offset:512
	v_mfma_f32_32x32x16_bf16 v[80:95], v[212:215], v[44:47], v[80:95]
	ds_read_b64_tr_b16 v[36:37], v13 offset:1024
	ds_read_b64_tr_b16 v[38:39], v13 offset:1536
	s_waitcnt lgkmcnt(10)
	v_mfma_f32_32x32x16_bf16 v[64:79], v[48:51], v[216:219], v[64:79]
	ds_read_b128 v[228:231], v10 offset:49152
	ds_read_b64_tr_b16 v[40:41], v14
	v_mfma_f32_32x32x16_bf16 v[80:95], v[216:219], v[52:55], v[80:95]
	ds_read_b64_tr_b16 v[42:43], v14 offset:512
	ds_read_b64_tr_b16 v[44:45], v14 offset:1024
	v_mfma_f32_16x16x32_bf16 v[96:99], v[28:31], v[48:51], v[96:99]
	ds_read_b64_tr_b16 v[46:47], v14 offset:1536
	v_mfma_f32_16x16x32_bf16 v[100:103], v[28:31], v[52:55], v[100:103]
	s_waitcnt lgkmcnt(10)
	v_mfma_f32_32x32x16_bf16 v[64:79], v[56:59], v[220:223], v[64:79]
	ds_read_b128 v[232:235], v11 offset:49152
	ds_read_b64_tr_b16 v[48:49], v13 offset:8192
	ds_read_b64_tr_b16 v[50:51], v13 offset:8704
	v_mfma_f32_32x32x16_bf16 v[80:95], v[220:223], v[60:63], v[80:95]
	ds_read_b64_tr_b16 v[52:53], v13 offset:9216
	ds_read_b64_tr_b16 v[54:55], v13 offset:9728
	s_waitcnt lgkmcnt(10)
	v_mfma_f32_32x32x16_bf16 v[64:79], v[32:35], v[224:227], v[64:79]
	ds_read_b128 v[236:239], v12 offset:49152
	ds_read_b64_tr_b16 v[56:57], v14 offset:8192
	v_mfma_f32_32x32x16_bf16 v[80:95], v[224:227], v[36:39], v[80:95]
	ds_read_b64_tr_b16 v[58:59], v14 offset:8704
	ds_read_b64_tr_b16 v[60:61], v14 offset:9216
	v_mfma_f32_16x16x32_bf16 v[96:99], v[28:31], v[32:35], v[96:99]
	ds_read_b64_tr_b16 v[62:63], v14 offset:9728
	v_mfma_f32_16x16x32_bf16 v[100:103], v[28:31], v[36:39], v[100:103]
	s_barrier
	s_waitcnt lgkmcnt(10)
	v_mfma_f32_32x32x16_bf16 v[64:79], v[40:43], v[228:231], v[64:79]
	ds_read_b128 v[240:243], v9 offset:57344
	ds_read_b64_tr_b16 v[32:33], v13 offset:16384
	ds_read_b64_tr_b16 v[34:35], v13 offset:16896
	v_mfma_f32_32x32x16_bf16 v[80:95], v[228:231], v[44:47], v[80:95]
	ds_read_b64_tr_b16 v[36:37], v13 offset:17408
	ds_read_b64_tr_b16 v[38:39], v13 offset:17920
	s_waitcnt lgkmcnt(10)
	v_mfma_f32_32x32x16_bf16 v[64:79], v[48:51], v[232:235], v[64:79]
	ds_read_b128 v[244:247], v10 offset:57344
	ds_read_b64_tr_b16 v[40:41], v14 offset:16384
	v_mfma_f32_32x32x16_bf16 v[80:95], v[232:235], v[52:55], v[80:95]
	ds_read_b64_tr_b16 v[42:43], v14 offset:16896
	ds_read_b64_tr_b16 v[44:45], v14 offset:17408
	v_mfma_f32_16x16x32_bf16 v[96:99], v[28:31], v[48:51], v[96:99]
	ds_read_b64_tr_b16 v[46:47], v14 offset:17920
	v_mfma_f32_16x16x32_bf16 v[100:103], v[28:31], v[52:55], v[100:103]
	s_waitcnt lgkmcnt(10)
	v_mfma_f32_32x32x16_bf16 v[64:79], v[56:59], v[236:239], v[64:79]
	ds_read_b128 v[248:251], v11 offset:57344
	ds_read_b64_tr_b16 v[48:49], v13 offset:24576
	ds_read_b64_tr_b16 v[50:51], v13 offset:25088
	v_mfma_f32_32x32x16_bf16 v[80:95], v[236:239], v[60:63], v[80:95]
	ds_read_b64_tr_b16 v[52:53], v13 offset:25600
	ds_read_b64_tr_b16 v[54:55], v13 offset:26112
	s_nop 3
	ds_write2st64_b32 v21, v96, v100 offset1:1
	v_xor_b32 v21, 0x200, v21
	s_waitcnt lgkmcnt(11)
	v_mfma_f32_32x32x16_bf16 v[64:79], v[32:35], v[240:243], v[64:79]
	ds_read_b128 v[252:255], v12 offset:57344
	ds_read_b64_tr_b16 v[56:57], v14 offset:24576
	v_mfma_f32_32x32x16_bf16 v[80:95], v[240:243], v[36:39], v[80:95]
	ds_read_b64_tr_b16 v[58:59], v14 offset:25088
	ds_read_b64_tr_b16 v[60:61], v14 offset:25600
	v_mfma_f32_16x16x32_bf16 v[112:115], v[28:31], v[32:35], v[96:99]
	ds_read_b64_tr_b16 v[62:63], v14 offset:26112
	v_mfma_f32_16x16x32_bf16 v[116:119], v[28:31], v[36:39], v[100:103]
	s_waitcnt lgkmcnt(11)
	v_mfma_f32_32x32x16_bf16 v[64:79], v[40:43], v[244:247], v[64:79]
	v_mfma_f32_32x32x16_bf16 v[80:95], v[244:247], v[44:47], v[80:95]
	v_mfma_f32_16x16x32_bf16 v[112:115], v[28:31], v[40:43], v[112:115]
	v_mfma_f32_16x16x32_bf16 v[116:119], v[28:31], v[44:47], v[116:119]
	s_waitcnt lgkmcnt(5)
	s_barrier
	ds_read2st64_b32 v[22:23], v21 offset1:1
	ds_read_b64 v[24:25], v4
	ds_read_b32 v7, v7 offset:512
	ds_read_b128 v[96:99], v1 offset:512
	ds_read_b128 v[100:103], v1 offset:544
	ds_read_b128 v[104:107], v1 offset:576
	ds_read_b128 v[108:111], v1 offset:608
	v_mfma_f32_32x32x16_bf16 v[64:79], v[48:51], v[248:251], v[64:79]
	v_mfma_f32_32x32x16_bf16 v[80:95], v[248:251], v[52:55], v[80:95]
	v_mfma_f32_16x16x32_bf16 v[112:115], v[28:31], v[48:51], v[112:115]
	v_mfma_f32_16x16x32_bf16 v[116:119], v[28:31], v[52:55], v[116:119]
	s_waitcnt lgkmcnt(7)
	v_mfma_f32_16x16x32_bf16 v[112:115], v[28:31], v[56:59], v[112:115]
	v_mfma_f32_16x16x32_bf16 v[116:119], v[28:31], v[60:63], v[116:119]
	v_mfma_f32_32x32x16_bf16 v[64:79], v[56:59], v[252:255], v[64:79]
	v_mfma_f32_32x32x16_bf16 v[80:95], v[252:255], v[60:63], v[80:95]
	s_waitcnt lgkmcnt(6)
	s_nop 7
	v_add_f32 v112, v112, v22
	v_cvt_pk_bf16_f32 v113, v112, 0
	v_lshlrev_b32 v114, 16, v113
	v_sub_f32 v114, v112, v114
	v_cvt_pk_bf16_f32 v112, v112, v114
	v_mov_b32 v114, 0
	v_mov_b32 v115, 0
	s_waitcnt lgkmcnt(0)
	s_nop 0
	v_mfma_f32_32x32x16_bf16 v[64:79], v[112:115], v[24:27], v[64:79]
	v_add_f32 v116, v116, v23
	v_cvt_pk_bf16_f32 v117, v116, 0
	v_lshlrev_b32 v118, 16, v117
	v_sub_f32 v118, v116, v118
	v_cvt_pk_bf16_f32 v116, v116, v118
	v_mov_b32 v118, 0
	v_mov_b32 v119, 0
	s_nop 1
	v_mfma_f32_32x32x16_bf16 v[80:95], v[24:27], v[116:119], v[80:95]
	v_mov_b32 v6, v7
	s_nop 0
	v_pk_mul_f32 v[64:65], v[64:65], v[6:7]
	v_pk_mul_f32 v[66:67], v[66:67], v[6:7]
	v_pk_mul_f32 v[68:69], v[68:69], v[6:7]
	v_pk_mul_f32 v[70:71], v[70:71], v[6:7]
	v_cvt_pk_bf16_f32 v32, v64, v65
	v_cvt_pk_bf16_f32 v33, v66, v67
	v_cvt_pk_bf16_f32 v34, v68, v69
	v_cvt_pk_bf16_f32 v35, v70, v71
	buffer_store_dwordx4 v[32:35], v17, s[12:15], s32 offen sc1
	v_pk_mul_f32 v[72:73], v[72:73], v[6:7]
	v_pk_mul_f32 v[74:75], v[74:75], v[6:7]
	v_pk_mul_f32 v[76:77], v[76:77], v[6:7]
	v_pk_mul_f32 v[78:79], v[78:79], v[6:7]
	v_cvt_pk_bf16_f32 v36, v72, v73
	v_cvt_pk_bf16_f32 v37, v74, v75
	v_cvt_pk_bf16_f32 v38, v76, v77
	v_cvt_pk_bf16_f32 v39, v78, v79
	buffer_store_dwordx4 v[36:39], v17, s[12:15], s33 offen sc1
	v_pk_mul_f32 v[80:81], v[80:81], v[96:97]
	v_pk_mul_f32 v[82:83], v[82:83], v[98:99]
	v_pk_mul_f32 v[84:85], v[84:85], v[100:101]
	v_pk_mul_f32 v[86:87], v[86:87], v[102:103]
	v_cvt_pk_bf16_f32 v40, v80, v81
	v_cvt_pk_bf16_f32 v41, v82, v83
	v_cvt_pk_bf16_f32 v42, v84, v85
	v_cvt_pk_bf16_f32 v43, v86, v87
	buffer_store_dwordx4 v[40:43], v18, s[12:15], s32 offen offset:1024 sc1
	v_pk_mul_f32 v[88:89], v[88:89], v[104:105]
	v_pk_mul_f32 v[90:91], v[90:91], v[106:107]
	v_pk_mul_f32 v[92:93], v[92:93], v[108:109]
	v_pk_mul_f32 v[94:95], v[94:95], v[110:111]
	v_cvt_pk_bf16_f32 v44, v88, v89
	v_cvt_pk_bf16_f32 v45, v90, v91
	v_cvt_pk_bf16_f32 v46, v92, v93
	v_cvt_pk_bf16_f32 v47, v94, v95
	buffer_store_dwordx4 v[44:47], v18, s[12:15], s32 offen offset:1536 sc1
	s_barrier
	ds_read_b64_tr_b16 v[48:49], v15
	ds_read_b64_tr_b16 v[50:51], v15 offset:512
	v_mul_f32 v22, v64, v64
	v_mul_f32 v23, v72, v72
	v_fmac_f32 v22, v65, v65
	v_fmac_f32 v23, v73, v73
	v_fmac_f32 v22, v66, v66
	ds_read_b64_tr_b16 v[52:53], v16
	ds_read_b64_tr_b16 v[54:55], v16 offset:512
	v_fmac_f32 v23, v74, v74
	v_fmac_f32 v22, v67, v67
	v_fmac_f32 v23, v75, v75
	v_fmac_f32 v22, v68, v68
	v_fmac_f32 v23, v76, v76
	ds_read_b64_tr_b16 v[56:57], v15 offset:4096
	ds_read_b64_tr_b16 v[58:59], v15 offset:4608
	v_fmac_f32 v22, v69, v69
	v_fmac_f32 v23, v77, v77
	v_fmac_f32 v22, v70, v70
	v_fmac_f32 v23, v78, v78
	v_fmac_f32 v22, v71, v71
	ds_read_b64_tr_b16 v[60:61], v16 offset:4096
	ds_read_b64_tr_b16 v[62:63], v16 offset:4608
	v_fmac_f32 v23, v79, v79
	v_and_b32 v14, 27, v0
	v_cmp_eq_u32 vcc, 16, v14
	v_permlane32_swap_b32 v22, v23
	v_add_f32 v22, v22, v23
	ds_read_b64_tr_b16 v[84:85], v15 offset:8192
	ds_read_b64_tr_b16 v[86:87], v15 offset:8704
	v_mov_b32 v17, 0x3f803f80
	v_and_b32 v18, 15, v0
	v_max_u32_dpp v22, v22, v22 quad_perm:[1,0,3,2] row_mask:0xf bank_mask:0xf
	v_cndmask_b32 v14, 0, v17, vcc
	v_lshlrev_b32 v18, 4, v18
	s_waitcnt lgkmcnt(8)
	v_mfma_f32_32x32x16_bf16 v[32:47], v[48:51], v[128:131], 0
	ds_read_b64_tr_b16 v[96:97], v16 offset:8192
	ds_read_b64_tr_b16 v[98:99], v16 offset:8704
	v_max_u32_dpp v22, v22, v22 quad_perm:[2,3,0,1] row_mask:0xf bank_mask:0xf
	v_lshl_or_b32 v122, v8, 8, v18
	v_mov_b32 v120, 0xdddd
	v_max_u32_dpp v22, v22, v22 row_half_mirror row_mask:0xf bank_mask:0xf
	s_and_b32 s50, s3, 3
	s_waitcnt lgkmcnt(8)
	v_mfma_f32_32x32x16_bf16 v[32:47], v[52:55], v[132:135], v[32:47]
	ds_read_b64_tr_b16 v[108:109], v15 offset:12288
	ds_read_b64_tr_b16 v[110:111], v15 offset:12800
	s_lshl_b32 s36, s29, 2
	v_max_u32_dpp v22, v22, v22 row_mirror row_mask:0xf bank_mask:0xf
	s_add_u32 s36, s36, s50
	s_lshr_b32 s37, s3, 2
	v_max_u32_dpp v22, v22, v22 row_bcast:15 row_mask:0xa bank_mask:0xf
	s_waitcnt lgkmcnt(8)
	v_mfma_f32_32x32x16_bf16 v[32:47], v[56:59], v[136:139], v[32:47]
	ds_read_b64_tr_b16 v[128:129], v16 offset:12288
	ds_read_b64_tr_b16 v[130:131], v16 offset:12800
	v_or_b32 v22, 1, v22
	s_mov_b32 exec_lo, 0x80000000
	s_mov_b32 exec_hi, 0x80000000
	ds_write_b32 v19, v22
	s_mov_b64 exec, -1
	s_lshl_b32 s38, s36, 17
	s_lshl_b32 s59, s37, 11
	s_add_u32 s38, s38, s59
	v_xor_b32 v15, 98304, v15
	v_xor_b32 v16, 98304, v16
	s_barrier
	s_waitcnt lgkmcnt(9)
	v_mfma_f32_32x32x16_bf16 v[32:47], v[60:63], v[140:143], v[32:47]
	ds_read_b64_tr_b16 v[132:133], v15 offset:24576
	v_mfma_f32_16x16x32_bf16 v[10:13], v[28:31], v[48:51], 0
	ds_read_b64_tr_b16 v[134:135], v15 offset:25088
	v_mfma_f32_16x16x32_bf16 v[10:13], v[28:31], v[52:55], v[10:13]
	v_mfma_f32_16x16x32_bf16 v[10:13], v[28:31], v[56:59], v[10:13]
	v_mfma_f32_16x16x32_bf16 v[10:13], v[28:31], v[60:63], v[10:13]
	s_lshl_b32 s56, s52, 12
	s_and_b32 s57, s28, 24
	s_lshl_b32 s57, s57, 12
	s_add_u32 s57, s57, s38
	v_mov_b32 v76, v14
	s_waitcnt lgkmcnt(9)
	v_mfma_f32_32x32x16_bf16 v[32:47], v[84:87], v[144:147], v[32:47]
	ds_read_b64_tr_b16 v[136:137], v16 offset:24576
	ds_read_b64_tr_b16 v[138:139], v16 offset:25088
	v_mov_b32 v77, v14
	v_mov_b32 v78, v14
	v_mov_b32 v79, v14
	v_mov_b32 v80, v14
	v_mov_b32 v81, v14
	s_waitcnt lgkmcnt(9)
	v_mfma_f32_32x32x16_bf16 v[32:47], v[96:99], v[148:151], v[32:47]
	ds_read_b64_tr_b16 v[140:141], v15 offset:28672
	ds_read_b64_tr_b16 v[142:143], v15 offset:29184
	v_mov_b32 v82, v14
	v_mov_b32 v83, v14
	s_lshl_b32 s59, s37, 15
	s_add_u32 s59, s59, s54
	s_lshl_b32 s32, s36, 10
	s_waitcnt lgkmcnt(9)
	v_mfma_f32_32x32x16_bf16 v[32:47], v[108:111], v[152:155], v[32:47]
	ds_read_b64_tr_b16 v[144:145], v16 offset:28672
	ds_read_b64_tr_b16 v[146:147], v16 offset:29184
	s_add_u32 s32, s32, s59
	s_add_u32 s32, s32, 4194304
	v_mov_b32 v88, v14
	v_mov_b32 v89, v14
	v_mov_b32 v90, v14
	s_waitcnt lgkmcnt(9)
	v_mfma_f32_32x32x16_bf16 v[32:47], v[128:131], v[156:159], v[32:47]
	ds_read_b64_tr_b16 v[148:149], v15 offset:32768
	v_mfma_f32_16x16x32_bf16 v[10:13], v[28:31], v[84:87], v[10:13]
	ds_read_b64_tr_b16 v[150:151], v15 offset:33280
	v_mfma_f32_16x16x32_bf16 v[10:13], v[28:31], v[96:99], v[10:13]
	v_mfma_f32_16x16x32_bf16 v[10:13], v[28:31], v[108:111], v[10:13]
	v_mfma_f32_16x16x32_bf16 v[10:13], v[28:31], v[128:131], v[10:13]
	v_mov_b32 v91, v14
	v_mov_b32 v92, v14
	v_mov_b32 v93, v14
	v_mov_b32 v94, v14
	v_mov_b32 v95, v14
	s_waitcnt lgkmcnt(8)
	v_mfma_f32_32x32x16_bf16 v[32:47], v[132:135], v[160:163], v[32:47]
	ds_read_b64_tr_b16 v[152:153], v16 offset:32768
	ds_read_b64_tr_b16 v[154:155], v16 offset:33280
	v_mov_b32 v100, v14
	v_mov_b32 v101, v14
	v_mov_b32 v102, v14
	v_mov_b32 v103, v14
	v_mov_b32 v104, v14
	s_waitcnt lgkmcnt(8)
	v_mfma_f32_32x32x16_bf16 v[32:47], v[136:139], v[164:167], v[32:47]
	ds_read_b64_tr_b16 v[156:157], v15 offset:36864
	ds_read_b64_tr_b16 v[158:159], v15 offset:37376
	v_mov_b32 v105, v14
	v_mov_b32 v106, v14
	v_mov_b32 v107, v14
	v_mov_b32 v112, v14
	v_mov_b32 v113, v14
	s_waitcnt lgkmcnt(8)
	v_mfma_f32_32x32x16_bf16 v[32:47], v[140:143], v[168:171], v[32:47]
	ds_read_b64_tr_b16 v[160:161], v16 offset:36864
	ds_read_b64_tr_b16 v[162:163], v16 offset:37376
	v_mov_b32 v114, v14
	v_mov_b32 v115, v14
	v_mov_b32 v116, v14
	v_mov_b32 v117, v14
	v_mov_b32 v118, v14
	v_xor_b32 v15, 98304, v15
	v_xor_b32 v16, 98304, v16
	s_waitcnt lgkmcnt(8)
	v_mfma_f32_32x32x16_bf16 v[32:47], v[144:147], v[172:175], v[32:47]
	ds_read_b64_tr_b16 v[164:165], v15 offset:16384
	v_mfma_f32_16x16x32_bf16 v[10:13], v[28:31], v[132:135], v[10:13]
	ds_read_b64_tr_b16 v[166:167], v15 offset:16896
	v_mfma_f32_16x16x32_bf16 v[10:13], v[28:31], v[136:139], v[10:13]
	v_mfma_f32_16x16x32_bf16 v[10:13], v[28:31], v[140:143], v[10:13]
	v_mfma_f32_16x16x32_bf16 v[10:13], v[28:31], v[144:147], v[10:13]
	v_mov_b32 v119, v14
	s_cmp_eq_u32 s37, 0
	s_cselect_b32 s58, s58, 1879048192
	s_add_u32 s33, s32, 2097152
	s_lshl_b32 s59, s29, 3
	s_add_u32 s59, s59, s3
	s_waitcnt lgkmcnt(8)
	v_mfma_f32_32x32x16_bf16 v[32:47], v[148:151], v[176:179], v[32:47]
	ds_read_b64_tr_b16 v[168:169], v16 offset:16384
	ds_read_b64_tr_b16 v[170:171], v16 offset:16896
	s_lshl_b32 s39, s59, 15
	s_add_u32 s39, s39, 15728640
	s_cmp_eq_u32 s29, 7
	s_cselect_b32 s39, 1879048192, s39
	s_lshl_b32 s30, s28, 3
	s_add_u32 s30, s30, s29
	s_waitcnt lgkmcnt(8)
	v_mfma_f32_32x32x16_bf16 v[32:47], v[152:155], v[180:183], v[32:47]
	ds_read_b64_tr_b16 v[172:173], v15 offset:20480
	ds_read_b64_tr_b16 v[174:175], v15 offset:20992
	s_lshl_b32 s30, s30, 10
	s_add_u32 s30, s30, 19922944
	s_mul_i32 s35, s3, 7
	s_add_u32 s35, s35, s29
	s_lshl_b32 s35, s35, 11
	s_waitcnt lgkmcnt(8)
	v_mfma_f32_32x32x16_bf16 v[32:47], v[156:159], v[184:187], v[32:47]
	ds_read_b64_tr_b16 v[176:177], v16 offset:20480
	ds_read_b64_tr_b16 v[178:179], v16 offset:20992
	s_mul_i32 s31, s28, 229376
	s_add_u32 s31, s31, 8388608
	s_add_u32 s35, s35, s31
	s_cmp_eq_u32 s29, 7
	s_cselect_b32 s35, 1879048192, s35
	s_add_u32 s45, s35, 114688
	s_waitcnt lgkmcnt(8)
	s_waitcnt vmcnt(0)
	s_barrier
	v_mfma_f32_32x32x16_bf16 v[32:47], v[160:163], v[188:191], v[32:47]
	ds_read_b64_tr_b16 v[180:181], v15 offset:24576
	v_mfma_f32_16x16x32_bf16 v[10:13], v[28:31], v[148:151], v[10:13]
	ds_read_b64_tr_b16 v[182:183], v15 offset:25088
	v_mfma_f32_16x16x32_bf16 v[10:13], v[28:31], v[152:155], v[10:13]
	v_mfma_f32_16x16x32_bf16 v[10:13], v[28:31], v[156:159], v[10:13]
	v_mfma_f32_16x16x32_bf16 v[10:13], v[28:31], v[160:163], v[10:13]
	s_cmp_eq_u32 s3, 0
	s_cselect_b32 s34, s30, 1879048192
	s_mul_i32 s59, s29, 229376
	s_add_u32 s59, s59, 17825792
	s_mul_i32 s41, s3, 28672
	s_add_u32 s41, s41, s59
	s_add_u32 s44, s56, s57
	v_and_b32 v127, 0x330, v121
	v_and_b32 v72, 64, v121
	v_lshl_or_b32 v127, v72, 1, v127
	v_lshrrev_b32 v72, 1, v121
	v_and_or_b32 v127, v72, 64, v127
	buffer_load_dwordx4 v[72:75], v127, s[12:15], s44 offen sc1
	s_mov_b64 exec, s[46:47]
	buffer_load_dwordx4 v[76:79], v122, s[12:15], s44 offen offset:1024 sc1
	buffer_load_dwordx4 v[80:83], v122, s[12:15], s44 offen offset:1536 sc1
	s_mov_b64 exec, -1
	s_waitcnt lgkmcnt(8)
	v_mfma_f32_32x32x16_bf16 v[32:47], v[164:167], v[192:195], v[32:47]
	ds_read_b64_tr_b16 v[184:185], v16 offset:24576
	ds_read_b64_tr_b16 v[186:187], v16 offset:25088
	s_and_b32 s53, s28, 7
	s_lshl_b32 s53, s53, 3
	s_add_u32 s53, s53, s3
	s_mul_i32 s53, s53, 3584
	s_add_u32 s53, s53, s59
	s_waitcnt lgkmcnt(8)
	v_mfma_f32_32x32x16_bf16 v[32:47], v[168:171], v[196:199], v[32:47]
	ds_read_b64_tr_b16 v[188:189], v15 offset:28672
	ds_read_b64_tr_b16 v[190:191], v15 offset:29184
	s_lshl_b32 s54, s28, 16
	s_lshl_b32 s42, s29, 7
	s_add_u32 s42, s42, s54
	s_lshl_b32 s51, s28, 7
	s_add_u32 s51, s51, 20217856
	s_waitcnt lgkmcnt(8)
	v_mfma_f32_32x32x16_bf16 v[32:47], v[172:175], v[200:203], v[32:47]
	ds_read_b64_tr_b16 v[192:193], v16 offset:28672
	ds_read_b64_tr_b16 v[194:195], v16 offset:29184
	s_cmp_eq_u32 s37, 0
	s_cselect_b32 s48, s29, s29
	s_cselect_b32 s59, s28, s28
	s_lshl_b32 s48, s48, 5
	s_add_u32 s48, s48, s59
	s_lshl_b32 s48, s48, 5
	s_add_u32 s48, s48, s3
	s_waitcnt lgkmcnt(8)
	v_mfma_f32_32x32x16_bf16 v[32:47], v[176:179], v[204:207], v[32:47]
	ds_read_b64_tr_b16 v[196:197], v15 offset:32768
	v_mfma_f32_16x16x32_bf16 v[10:13], v[28:31], v[164:167], v[10:13]
	ds_read_b64_tr_b16 v[198:199], v15 offset:33280
	v_mfma_f32_16x16x32_bf16 v[10:13], v[28:31], v[168:171], v[10:13]
	v_mfma_f32_16x16x32_bf16 v[10:13], v[28:31], v[172:175], v[10:13]
	v_mfma_f32_16x16x32_bf16 v[10:13], v[28:31], v[176:179], v[10:13]
	s_lshl_b32 s48, s48, 2
	s_add_u32 s48, s48, 20217856
	v_lshrrev_b32 v18, 3, v126
	v_lshlrev_b32 v17, 2, v3
	v_lshl_or_b32 v124, v18, 12, v17
	s_waitcnt lgkmcnt(8)
	v_mfma_f32_32x32x16_bf16 v[32:47], v[180:183], v[208:211], v[32:47]
	ds_read_b64_tr_b16 v[200:201], v16 offset:32768
	ds_read_b64_tr_b16 v[202:203], v16 offset:33280
	v_min_u32 v125, 27, v126
	v_lshlrev_b32 v125, 7, v125
	s_lshl_b32 s59, s50, 11
	s_lshl_b32 s62, s37, 10
	s_add_u32 s59, s59, s62
	s_waitcnt lgkmcnt(8)
	v_mfma_f32_32x32x16_bf16 v[32:47], v[184:187], v[212:215], v[32:47]
	ds_read_b64_tr_b16 v[204:205], v15 offset:36864
	ds_read_b64_tr_b16 v[206:207], v15 offset:37376
	s_add_u32 s59, s59, 81920
	v_add_u32 v64, s59, v121
	v_xor_b32 v68, 0x400, v64
	s_mul_i32 s62, s3, 2
	s_lshr_b32 s62, s62, 3
	s_waitcnt lgkmcnt(8)
	v_mfma_f32_32x32x16_bf16 v[32:47], v[188:191], v[216:219], v[32:47]
	ds_read_b64_tr_b16 v[208:209], v16 offset:36864
	ds_read_b64_tr_b16 v[210:211], v16 offset:37376
	v_mov_b32 v0, 0
	v_mov_b32 v1, 0
	v_mov_b32 v2, 0
	v_mov_b32 v3, 0
	v_mov_b32 v4, 0
	s_waitcnt lgkmcnt(8)
	v_mfma_f32_32x32x16_bf16 v[32:47], v[192:195], v[220:223], v[32:47]
	ds_read_b64_tr_b16 v[212:213], v15 offset:40960
	v_mfma_f32_16x16x32_bf16 v[10:13], v[28:31], v[180:183], v[10:13]
	ds_read_b64_tr_b16 v[214:215], v15 offset:41472
	v_mfma_f32_16x16x32_bf16 v[10:13], v[28:31], v[184:187], v[10:13]
	v_mfma_f32_16x16x32_bf16 v[10:13], v[28:31], v[188:191], v[10:13]
	v_mfma_f32_16x16x32_bf16 v[10:13], v[28:31], v[192:195], v[10:13]
	v_mov_b32 v5, 0
	v_mov_b32 v7, 0
	v_mov_b32 v8, 0
	v_mov_b32 v9, 0
	v_mov_b32 v14, 0
	s_waitcnt lgkmcnt(8)
	v_mfma_f32_32x32x16_bf16 v[32:47], v[196:199], v[224:227], v[32:47]
	ds_read_b64_tr_b16 v[216:217], v16 offset:40960
	ds_read_b64_tr_b16 v[218:219], v16 offset:41472
	v_mov_b32 v17, 0
	v_mov_b32 v18, 0
	v_mov_b32 v19, 0
	v_mov_b32 v21, 0
	v_mov_b32 v22, 0
	s_waitcnt lgkmcnt(8)
	v_mfma_f32_32x32x16_bf16 v[32:47], v[200:203], v[228:231], v[32:47]
	ds_read_b64_tr_b16 v[220:221], v15 offset:45056
	ds_read_b64_tr_b16 v[222:223], v15 offset:45568
	v_mov_b32 v23, 0
	s_waitcnt lgkmcnt(8)
	v_mfma_f32_32x32x16_bf16 v[32:47], v[204:207], v[232:235], v[32:47]
	ds_read_b64_tr_b16 v[224:225], v16 offset:45056
	ds_read_b64_tr_b16 v[226:227], v16 offset:45568
	s_waitcnt lgkmcnt(8)
	v_mfma_f32_32x32x16_bf16 v[32:47], v[208:211], v[236:239], v[32:47]
	v_mfma_f32_16x16x32_bf16 v[10:13], v[28:31], v[196:199], v[10:13]
	v_mfma_f32_16x16x32_bf16 v[10:13], v[28:31], v[200:203], v[10:13]
	v_mfma_f32_16x16x32_bf16 v[10:13], v[28:31], v[204:207], v[10:13]
	v_mfma_f32_16x16x32_bf16 v[10:13], v[28:31], v[208:211], v[10:13]
	s_waitcnt lgkmcnt(6)
	v_mfma_f32_32x32x16_bf16 v[32:47], v[212:215], v[240:243], v[32:47]
	s_waitcnt lgkmcnt(4)
	v_mfma_f32_32x32x16_bf16 v[32:47], v[216:219], v[244:247], v[32:47]
	s_waitcnt lgkmcnt(2)
	v_mfma_f32_32x32x16_bf16 v[32:47], v[220:223], v[248:251], v[32:47]
	s_waitcnt lgkmcnt(0)
	v_mfma_f32_16x16x32_bf16 v[10:13], v[28:31], v[212:215], v[10:13]
	v_mfma_f32_16x16x32_bf16 v[10:13], v[28:31], v[216:219], v[10:13]
	v_mfma_f32_16x16x32_bf16 v[10:13], v[28:31], v[220:223], v[10:13]
	v_mfma_f32_16x16x32_bf16 v[10:13], v[28:31], v[224:227], v[10:13]
	v_mfma_f32_32x32x16_bf16 v[32:47], v[224:227], v[252:255], v[32:47]
	s_nop 10
	v_cvt_pk_bf16_f32 v11, v10, 0
	v_lshlrev_b32 v12, 16, v11
	v_sub_f32 v12, v10, v12
	v_cvt_pk_bf16_f32 v10, v10, v12
	v_mov_b32 v12, 0
	v_mov_b32 v13, 0
	s_nop 1
	v_mfma_f32_32x32x16_bf16 v[32:47], v[10:13], v[24:27], v[32:47]
	v_mul_f32 v6, 0x3eb8aa3b, v6
	v_mov_b32 v252, -1
	v_mov_b32 v253, -1
	v_mov_b32 v254, -1
	v_mov_b32 v255, -1
	v_mov_b32 v15, 0
	v_mov_b32 v16, 0
	v_mov_b32 v28, 0
	v_mov_b32 v29, 0
	v_mov_b32 v30, 0
	v_mov_b32 v31, 0
	v_mov_b32 v10, 0
	v_mov_b32 v11, 0
	v_mov_b32 v12, 0
	v_mov_b32 v13, 0
	v_mov_b32 v24, 0
	v_mov_b32 v25, 0
	v_mov_b32 v26, 0
	v_mov_b32 v27, 0
	v_mul_f32 v32, v6, v32
	v_mul_f32 v33, v6, v33
	v_mul_f32 v34, v6, v34
	v_mul_f32 v35, v6, v35
	v_mul_f32 v36, v6, v36
	v_mul_f32 v37, v6, v37
	v_mul_f32 v38, v6, v38
	v_mul_f32 v39, v6, v39
	v_cvt_pk_bf16_f32 v48, v32, v33
	v_cvt_pk_bf16_f32 v49, v34, v35
	v_cvt_pk_bf16_f32 v50, v36, v37
	v_cvt_pk_bf16_f32 v51, v38, v39
	ds_write_b128 v20, v[48:51]
	v_mul_f32 v40, v6, v40
	v_mul_f32 v41, v6, v41
	v_mul_f32 v42, v6, v42
	v_mul_f32 v43, v6, v43
	v_mul_f32 v44, v6, v44
	v_mul_f32 v45, v6, v45
	v_mul_f32 v46, v6, v46
	v_mul_f32 v47, v6, v47
	v_cvt_pk_bf16_f32 v52, v40, v41
	v_cvt_pk_bf16_f32 v53, v42, v43
	v_cvt_pk_bf16_f32 v54, v44, v45
	v_cvt_pk_bf16_f32 v55, v46, v47
	ds_write_b128 v20, v[52:55] offset:2048
	v_mov_b32 v6, 0
	v_mov_b32 v20, 0
.Lk_b12:
	s_waitcnt lgkmcnt(0)
	s_barrier
	s_add_u32 s56, s56, 4096
	s_and_b32 s56, s56, 0x7fff
	s_add_u32 s44, s56, s57
	buffer_load_dwordx4 v[84:87], v127, s[12:15], s44 offen sc1
	ds_read_b128 v[64:67], v64
	ds_read_b128 v[68:71], v68
	s_mov_b64 exec, s[46:47]
	buffer_load_dwordx4 v[88:91], v122, s[12:15], s44 offen offset:1024 sc1
	buffer_load_dwordx4 v[92:95], v122, s[12:15], s44 offen offset:1536 sc1
	s_mov_b64 exec, -1
	s_waitcnt vmcnt(5)
	s_waitcnt lgkmcnt(1)
	v_mfma_f32_32x32x16_bf16 v[32:47], v[72:75], v[64:67], 0
	s_waitcnt lgkmcnt(0)
	s_setprio 0
	s_nop 9
	v_exp_f32 v32, v32
	s_setprio 1
	v_exp_f32 v33, v33
	v_exp_f32 v34, v34
	v_exp_f32 v35, v35
	v_exp_f32 v36, v36
	v_exp_f32 v37, v37
	v_mfma_f32_32x32x16_bf16 v[48:63], v[72:75], v[68:71], 0
	s_add_u32 s56, s56, 4096
	s_and_b32 s56, s56, 0x7fff
	s_add_u32 s44, s56, s57
	buffer_load_dwordx4 v[96:99], v127, s[12:15], s44 offen sc1
	v_exp_f32 v38, v38
	v_exp_f32 v39, v39
	v_exp_f32 v40, v40
	v_exp_f32 v41, v41
	s_setprio 2
	v_exp_f32 v42, v42
	v_exp_f32 v43, v43
	v_exp_f32 v44, v44
	v_exp_f32 v45, v45
	v_exp_f32 v46, v46
	v_exp_f32 v47, v47
	v_dot2_f32_bf16 v144, v64, v64, 0
	v_dot2_f32_bf16 v145, v68, v68, 0
	v_dot2_f32_bf16 v144, v65, v65, v144
	v_dot2_f32_bf16 v145, v69, v69, v145
	v_dot2_f32_bf16 v144, v66, v66, v144
	v_dot2_f32_bf16 v145, v70, v70, v145
	v_dot2_f32_bf16 v144, v67, v67, v144
	v_dot2_f32_bf16 v145, v71, v71, v145
	v_lshrrev_b32 v149, 1, v126
	v_and_b32 v148, 16, v121
	v_lshrrev_b32 v150, 1, v148
	v_permlane32_swap_b32 v144, v145
	v_add_f32 v144, v144, v145
	v_lshl_or_b32 v148, v149, 7, v148
	s_and_b32 s59, s3, 3
	v_max_u32_dpp v144, v144, v144 quad_perm:[1,0,3,2] row_mask:0xf bank_mask:0xf
	v_lshl_or_b32 v148, s59, 2, v148
	s_lshl_b32 s26, s29, 12
	v_max_u32_dpp v144, v144, v144 quad_perm:[2,3,0,1] row_mask:0xf bank_mask:0xf
	v_lshl_or_b32 v149, v149, 7, v150
	s_lshl_b32 s27, s29, 12
	v_max_u32_dpp v144, v144, v144 row_half_mirror row_mask:0xf bank_mask:0xf
	s_add_u32 s26, s26, 20217856
	s_add_u32 s27, s27, 20185088
	v_max_u32_dpp v144, v144, v144 row_mirror row_mask:0xf bank_mask:0xf
	s_nop 1
	v_max_u32_dpp v144, v144, v144 row_bcast:15 row_mask:0xa bank_mask:0xf
	s_nop 1
	v_max_u32_dpp v144, v144, v144 row_bcast:31 row_mask:0xc bank_mask:0xf
	s_nop 0
	v_readlane_b32 s63, v144, 63
	s_waitcnt vmcnt(4)
	v_smfmac_f32_32x32x32_bf16 v[0:15], v[76:79], v[32:39], v120
	v_exp_f32 v48, v48
	v_exp_f32 v49, v49
	v_exp_f32 v50, v50
	v_smfmac_f32_32x32x32_bf16 v[0:15], v[80:83], v[40:47], v120
	v_exp_f32 v51, v51
	v_exp_f32 v52, v52
	v_exp_f32 v53, v53
	s_mov_b64 exec, s[46:47]
	buffer_load_dwordx4 v[100:103], v122, s[12:15], s44 offen offset:1024 sc1
	buffer_load_dwordx4 v[104:107], v122, s[12:15], s44 offen offset:1536 sc1
	s_mov_b64 exec, -1
	v_exp_f32 v54, v54
	v_exp_f32 v55, v55
	v_exp_f32 v56, v56
	v_exp_f32 v57, v57
	v_exp_f32 v58, v58
	v_exp_f32 v59, v59
	v_exp_f32 v60, v60
	s_waitcnt vmcnt(5)
	v_mfma_f32_32x32x16_bf16 v[32:47], v[84:87], v[64:67], 0
	v_exp_f32 v61, v61
	v_exp_f32 v62, v62
	v_exp_f32 v63, v63
	v_smfmac_f32_32x32x32_bf16 v[16:31], v[76:79], v[48:55], v120
	s_setprio 0
	s_nop 6
	v_exp_f32 v32, v32
	s_setprio 1
	v_exp_f32 v33, v33
	v_exp_f32 v34, v34
	v_smfmac_f32_32x32x32_bf16 v[16:31], v[80:83], v[56:63], v120
	s_add_u32 s56, s56, 4096
	s_and_b32 s56, s56, 0x7fff
	s_add_u32 s44, s56, s57
	buffer_load_dwordx4 v[108:111], v127, s[12:15], s44 offen sc1
	s_mov_b64 exec, s[46:47]
	buffer_load_dwordx4 v[112:115], v122, s[12:15], s44 offen offset:1024 sc1
	buffer_load_dwordx4 v[116:119], v122, s[12:15], s44 offen offset:1536 sc1
	s_mov_b64 exec, -1
	v_exp_f32 v35, v35
	v_exp_f32 v36, v36
	v_exp_f32 v37, v37
	v_mfma_f32_32x32x16_bf16 v[48:63], v[84:87], v[68:71], 0
	s_add_u32 s56, s56, 4096
	s_and_b32 s56, s56, 0x7fff
	s_add_u32 s44, s56, s57
	buffer_load_dwordx4 v[72:75], v127, s[12:15], s44 offen sc1
	v_exp_f32 v38, v38
	v_exp_f32 v39, v39
	v_exp_f32 v40, v40
	v_exp_f32 v41, v41
	s_setprio 2
	v_exp_f32 v42, v42
	v_exp_f32 v43, v43
	v_exp_f32 v44, v44
	v_exp_f32 v45, v45
	v_exp_f32 v46, v46
	v_exp_f32 v47, v47
	s_waitcnt vmcnt(6)
	v_smfmac_f32_32x32x32_bf16 v[0:15], v[88:91], v[32:39], v120
	v_exp_f32 v48, v48
	v_exp_f32 v49, v49
	v_exp_f32 v50, v50
	v_smfmac_f32_32x32x32_bf16 v[0:15], v[92:95], v[40:47], v120
	v_exp_f32 v51, v51
	v_exp_f32 v52, v52
	v_exp_f32 v53, v53
	v_mfma_f32_32x32x16_bf16 v[32:47], v[96:99], v[64:67], 0
	s_mov_b64 exec, s[46:47]
	buffer_load_dwordx4 v[76:79], v122, s[12:15], s44 offen offset:1024 sc1
	buffer_load_dwordx4 v[80:83], v122, s[12:15], s44 offen offset:1536 sc1
	s_mov_b64 exec, -1
	s_cmp_lt_u32 s3, 4
	s_cselect_b32 s59, s48, 1879048192
	s_cselect_b32 s24, 1879048192, s32
	s_mov_b64 exec, 1
	buffer_store_dword v252, off, s[12:15], s59 sc1
	s_mov_b64 exec, -1
	v_exp_f32 v54, v54
	v_exp_f32 v55, v55
	v_exp_f32 v56, v56
	v_exp_f32 v57, v57
	v_exp_f32 v58, v58
	v_exp_f32 v59, v59
	v_exp_f32 v60, v60
	v_exp_f32 v61, v61
	v_exp_f32 v62, v62
	v_exp_f32 v63, v63
	v_smfmac_f32_32x32x32_bf16 v[16:31], v[88:91], v[48:55], v120
	s_setprio 0
	v_exp_f32 v32, v32
	s_setprio 1
	v_exp_f32 v33, v33
	v_exp_f32 v34, v34
	v_smfmac_f32_32x32x32_bf16 v[16:31], v[92:95], v[56:63], v120
	s_add_u32 s56, s56, 4096
	s_and_b32 s56, s56, 0x7fff
	s_add_u32 s44, s56, s57
	buffer_load_dwordx4 v[84:87], v127, s[12:15], s44 offen sc1
	s_mov_b64 exec, s[46:47]
	buffer_load_dwordx4 v[88:91], v122, s[12:15], s44 offen offset:1024 sc1
	buffer_load_dwordx4 v[92:95], v122, s[12:15], s44 offen offset:1536 sc1
	s_mov_b64 exec, -1
	v_exp_f32 v35, v35
	v_exp_f32 v36, v36
	v_exp_f32 v37, v37
	v_mfma_f32_32x32x16_bf16 v[48:63], v[96:99], v[68:71], 0
	s_add_u32 s56, s56, 4096
	s_and_b32 s56, s56, 0x7fff
	s_add_u32 s44, s56, s57
	buffer_load_dwordx4 v[96:99], v127, s[12:15], s44 offen sc1
	v_exp_f32 v38, v38
	v_exp_f32 v39, v39
	v_exp_f32 v40, v40
	v_exp_f32 v41, v41
	s_setprio 2
	v_exp_f32 v42, v42
	v_exp_f32 v43, v43
	v_exp_f32 v44, v44
	v_exp_f32 v45, v45
	v_exp_f32 v46, v46
	v_exp_f32 v47, v47
	s_waitcnt vmcnt(10)
	v_smfmac_f32_32x32x32_bf16 v[0:15], v[100:103], v[32:39], v120
	v_exp_f32 v48, v48
	v_exp_f32 v49, v49
	v_exp_f32 v50, v50
	v_smfmac_f32_32x32x32_bf16 v[0:15], v[104:107], v[40:47], v120
	v_exp_f32 v51, v51
	v_exp_f32 v52, v52
	v_exp_f32 v53, v53
	v_mfma_f32_32x32x16_bf16 v[32:47], v[108:111], v[64:67], 0
	s_cmp_lg_u32 s58, 1879048192
	s_cbranch_scc1 .Lk_la
.Lk_lar:
	buffer_load_dword v144, v148, s[12:15], s27 offen sc1
	v_exp_f32 v54, v54
	v_exp_f32 v55, v55
	v_exp_f32 v56, v56
	v_exp_f32 v57, v57
	v_exp_f32 v58, v58
	v_exp_f32 v59, v59
	v_exp_f32 v60, v60
	v_exp_f32 v61, v61
	v_exp_f32 v62, v62
	v_exp_f32 v63, v63
	v_smfmac_f32_32x32x32_bf16 v[16:31], v[100:103], v[48:55], v120
	s_setprio 0
	v_exp_f32 v32, v32
	s_setprio 1
	v_exp_f32 v33, v33
	v_exp_f32 v34, v34
	v_smfmac_f32_32x32x32_bf16 v[16:31], v[104:107], v[56:63], v120
	s_mov_b64 exec, s[46:47]
	buffer_load_dwordx4 v[100:103], v122, s[12:15], s44 offen offset:1024 sc1
	buffer_load_dwordx4 v[104:107], v122, s[12:15], s44 offen offset:1536 sc1
	s_mov_b64 exec, -1
	v_exp_f32 v35, v35
	v_exp_f32 v36, v36
	v_exp_f32 v37, v37
	v_mfma_f32_32x32x16_bf16 v[48:63], v[108:111], v[68:71], 0
	s_add_u32 s56, s56, 4096
	s_and_b32 s56, s56, 0x7fff
	s_add_u32 s44, s56, s57
	buffer_load_dwordx4 v[108:111], v127, s[12:15], s44 offen sc1
	v_exp_f32 v38, v38
	v_exp_f32 v39, v39
	v_exp_f32 v40, v40
	v_exp_f32 v41, v41
	s_setprio 2
	v_exp_f32 v42, v42
	v_exp_f32 v43, v43
	v_exp_f32 v44, v44
	v_exp_f32 v45, v45
	v_exp_f32 v46, v46
	v_exp_f32 v47, v47
	s_waitcnt vmcnt(11)
	v_smfmac_f32_32x32x32_bf16 v[0:15], v[112:115], v[32:39], v120
	v_exp_f32 v48, v48
	v_exp_f32 v49, v49
	v_exp_f32 v50, v50
	v_smfmac_f32_32x32x32_bf16 v[0:15], v[116:119], v[40:47], v120
	v_exp_f32 v51, v51
	v_exp_f32 v52, v52
	v_exp_f32 v53, v53
	v_mfma_f32_32x32x16_bf16 v[32:47], v[72:75], v[64:67], 0
	buffer_store_dwordx4 v[252:255], v121, s[12:15], s34 offen sc1
	v_exp_f32 v54, v54
	v_exp_f32 v55, v55
	v_exp_f32 v56, v56
	v_exp_f32 v57, v57
	v_exp_f32 v58, v58
	v_exp_f32 v59, v59
	v_exp_f32 v60, v60
	v_exp_f32 v61, v61
	v_exp_f32 v62, v62
	v_exp_f32 v63, v63
	s_waitcnt vmcnt(4)
	s_cmp_lg_u32 s58, 1879048192
	s_cbranch_scc1 .Lk_beat
.Lk_ev2:
	v_cmp_eq_u32 vcc, 0, v144
	v_mul_f32 v145, s63, v144
	s_cbranch_vccnz .Lk_re2
	v_cmp_nge_f32 vcc, 0x45100000, v145
	s_cbranch_vccnz .Lk_slow
	v_smfmac_f32_32x32x32_bf16 v[16:31], v[112:115], v[48:55], v120
	s_setprio 0
	v_exp_f32 v32, v32
	s_setprio 1
	v_exp_f32 v33, v33
	v_exp_f32 v34, v34
	v_smfmac_f32_32x32x32_bf16 v[16:31], v[116:119], v[56:63], v120
	s_mov_b64 exec, s[46:47]
	buffer_load_dwordx4 v[112:115], v122, s[12:15], s44 offen offset:1024 sc1
	buffer_load_dwordx4 v[116:119], v122, s[12:15], s44 offen offset:1536 sc1
	s_mov_b64 exec, -1
	v_exp_f32 v35, v35
	v_exp_f32 v36, v36
	v_exp_f32 v37, v37
	v_mfma_f32_32x32x16_bf16 v[48:63], v[72:75], v[68:71], 0
	s_add_u32 s56, s56, 4096
	s_and_b32 s56, s56, 0x7fff
	s_add_u32 s44, s57, 32768
	s_and_b32 s44, s44, 0x18000
	s_add_u32 s57, s38, s44
	s_add_u32 s44, s56, s57
	buffer_load_dwordx4 v[72:75], v127, s[12:15], s44 offen sc1
	v_exp_f32 v38, v38
	v_exp_f32 v39, v39
	v_exp_f32 v40, v40
	v_exp_f32 v41, v41
	s_setprio 2
	v_exp_f32 v42, v42
	v_exp_f32 v43, v43
	v_exp_f32 v44, v44
	v_exp_f32 v45, v45
	v_exp_f32 v46, v46
	v_exp_f32 v47, v47
	v_smfmac_f32_32x32x32_bf16 v[0:15], v[76:79], v[32:39], v120
	v_exp_f32 v48, v48
	v_exp_f32 v49, v49
	v_exp_f32 v50, v50
	v_smfmac_f32_32x32x32_bf16 v[0:15], v[80:83], v[40:47], v120
	v_exp_f32 v51, v51
	v_exp_f32 v52, v52
	v_exp_f32 v53, v53
	v_mfma_f32_32x32x16_bf16 v[32:47], v[84:87], v[64:67], 0
	v_exp_f32 v54, v54
	v_exp_f32 v55, v55
	v_exp_f32 v56, v56
	v_exp_f32 v57, v57
	v_exp_f32 v58, v58
	v_exp_f32 v59, v59
	v_exp_f32 v60, v60
	v_exp_f32 v61, v61
	v_exp_f32 v62, v62
	v_exp_f32 v63, v63
	v_smfmac_f32_32x32x32_bf16 v[16:31], v[76:79], v[48:55], v120
	s_setprio 0
	v_exp_f32 v32, v32
	s_setprio 1
	v_exp_f32 v33, v33
	v_exp_f32 v34, v34
	v_smfmac_f32_32x32x32_bf16 v[16:31], v[80:83], v[56:63], v120
	s_mov_b64 exec, s[46:47]
	buffer_load_dwordx4 v[76:79], v122, s[12:15], s44 offen offset:1024 sc1
	buffer_load_dwordx4 v[80:83], v122, s[12:15], s44 offen offset:1536 sc1
	s_mov_b64 exec, -1
	v_exp_f32 v35, v35
	v_exp_f32 v36, v36
	v_exp_f32 v37, v37
	v_mfma_f32_32x32x16_bf16 v[48:63], v[84:87], v[68:71], 0
	s_add_u32 s56, s56, 4096
	s_and_b32 s56, s56, 0x7fff
	s_add_u32 s44, s56, s57
	buffer_load_dwordx4 v[84:87], v127, s[12:15], s44 offen sc1
	v_exp_f32 v38, v38
	v_exp_f32 v39, v39
	v_exp_f32 v40, v40
	v_exp_f32 v41, v41
	s_setprio 2
	v_exp_f32 v42, v42
	v_exp_f32 v43, v43
	v_exp_f32 v44, v44
	v_exp_f32 v45, v45
	v_exp_f32 v46, v46
	v_exp_f32 v47, v47
	v_smfmac_f32_32x32x32_bf16 v[0:15], v[88:91], v[32:39], v120
	v_exp_f32 v48, v48
	v_exp_f32 v49, v49
	v_exp_f32 v50, v50
	v_smfmac_f32_32x32x32_bf16 v[0:15], v[92:95], v[40:47], v120
	v_exp_f32 v51, v51
	v_exp_f32 v52, v52
	v_exp_f32 v53, v53
	v_mfma_f32_32x32x16_bf16 v[32:47], v[96:99], v[64:67], 0
	v_exp_f32 v54, v54
	v_exp_f32 v55, v55
	v_exp_f32 v56, v56
	v_exp_f32 v57, v57
	v_exp_f32 v58, v58
	v_exp_f32 v59, v59
	v_exp_f32 v60, v60
	v_exp_f32 v61, v61
	v_exp_f32 v62, v62
	v_exp_f32 v63, v63
	v_smfmac_f32_32x32x32_bf16 v[16:31], v[88:91], v[48:55], v120
	s_setprio 0
	v_exp_f32 v32, v32
	s_setprio 1
	v_exp_f32 v33, v33
	v_exp_f32 v34, v34
	v_smfmac_f32_32x32x32_bf16 v[16:31], v[92:95], v[56:63], v120
	s_mov_b64 exec, s[46:47]
	buffer_load_dwordx4 v[88:91], v122, s[12:15], s44 offen offset:1024 sc1
	buffer_load_dwordx4 v[92:95], v122, s[12:15], s44 offen offset:1536 sc1
	s_mov_b64 exec, -1
	v_exp_f32 v35, v35
	v_exp_f32 v36, v36
	v_exp_f32 v37, v37
	v_mfma_f32_32x32x16_bf16 v[48:63], v[96:99], v[68:71], 0
	s_add_u32 s56, s56, 4096
	s_and_b32 s56, s56, 0x7fff
	s_add_u32 s44, s56, s57
	buffer_load_dwordx4 v[96:99], v127, s[12:15], s44 offen sc1
	v_exp_f32 v38, v38
	v_exp_f32 v39, v39
	v_exp_f32 v40, v40
	v_exp_f32 v41, v41
	s_setprio 2
	v_exp_f32 v42, v42
	v_exp_f32 v43, v43
	v_exp_f32 v44, v44
	v_exp_f32 v45, v45
	v_exp_f32 v46, v46
	v_exp_f32 v47, v47
	s_waitcnt vmcnt(10)
	v_smfmac_f32_32x32x32_bf16 v[0:15], v[100:103], v[32:39], v120
	v_exp_f32 v48, v48
	v_exp_f32 v49, v49
	v_exp_f32 v50, v50
	v_smfmac_f32_32x32x32_bf16 v[0:15], v[104:107], v[40:47], v120
	v_exp_f32 v51, v51
	v_exp_f32 v52, v52
	v_exp_f32 v53, v53
	v_mfma_f32_32x32x16_bf16 v[32:47], v[108:111], v[64:67], 0
	buffer_load_dwordx2 v[144:145], v149, s[12:15], s26 offen sc1
	s_cmp_lg_u32 s62, 0
	s_cbranch_scc1 .Lk_f4
	buffer_store_dwordx4 v[252:255], v121, s[12:15], s33 offen sc1
.Lk_f4:
	v_exp_f32 v54, v54
	v_exp_f32 v55, v55
	v_exp_f32 v56, v56
	v_exp_f32 v57, v57
	v_exp_f32 v58, v58
	v_exp_f32 v59, v59
	v_exp_f32 v60, v60
	v_exp_f32 v61, v61
	v_exp_f32 v62, v62
	v_exp_f32 v63, v63
	v_smfmac_f32_32x32x32_bf16 v[16:31], v[100:103], v[48:55], v120
	s_setprio 0
	v_exp_f32 v32, v32
	s_setprio 1
	v_exp_f32 v33, v33
	v_exp_f32 v34, v34
	v_smfmac_f32_32x32x32_bf16 v[16:31], v[104:107], v[56:63], v120
	s_mov_b64 exec, s[46:47]
	buffer_load_dwordx4 v[100:103], v122, s[12:15], s44 offen offset:1024 sc1
	buffer_load_dwordx4 v[104:107], v122, s[12:15], s44 offen offset:1536 sc1
	s_mov_b64 exec, -1
	v_exp_f32 v35, v35
	v_exp_f32 v36, v36
	v_exp_f32 v37, v37
	v_mfma_f32_32x32x16_bf16 v[48:63], v[108:111], v[68:71], 0
	s_add_u32 s56, s56, 4096
	s_and_b32 s56, s56, 0x7fff
	s_add_u32 s44, s56, s57
	buffer_load_dwordx4 v[108:111], v127, s[12:15], s44 offen sc1
	v_exp_f32 v38, v38
	v_exp_f32 v39, v39
	v_exp_f32 v40, v40
	v_exp_f32 v41, v41
	s_setprio 2
	v_exp_f32 v42, v42
	v_exp_f32 v43, v43
	v_exp_f32 v44, v44
	v_exp_f32 v45, v45
	v_exp_f32 v46, v46
	v_exp_f32 v47, v47
	s_waitcnt vmcnt(10)
	v_smfmac_f32_32x32x32_bf16 v[0:15], v[112:115], v[32:39], v120
	v_exp_f32 v48, v48
	v_exp_f32 v49, v49
	v_exp_f32 v50, v50
	v_smfmac_f32_32x32x32_bf16 v[0:15], v[116:119], v[40:47], v120
	v_exp_f32 v51, v51
	v_exp_f32 v52, v52
	v_exp_f32 v53, v53
	v_mfma_f32_32x32x16_bf16 v[32:47], v[72:75], v[64:67], 0
	s_waitcnt vmcnt(3)
.Lk_e6:
	v_min_u32 v144, v144, v145
	v_cmp_eq_u32 vcc, 0, v144
	s_cbranch_vccnz .Lk_e6r
	buffer_load_dwordx4 v[128:131], v121, s[12:15], s39 offen sc1
	s_cmp_lg_u32 s62, 1
	s_cbranch_scc1 .Lk_f5
	buffer_store_dwordx4 v[252:255], v121, s[12:15], s33 offen sc1
.Lk_f5:
	v_exp_f32 v54, v54
	v_exp_f32 v55, v55
	v_exp_f32 v56, v56
	v_exp_f32 v57, v57
	v_exp_f32 v58, v58
	v_exp_f32 v59, v59
	v_exp_f32 v60, v60
	v_exp_f32 v61, v61
	v_exp_f32 v62, v62
	v_exp_f32 v63, v63
	v_smfmac_f32_32x32x32_bf16 v[16:31], v[112:115], v[48:55], v120
	s_setprio 0
	v_exp_f32 v32, v32
	s_setprio 1
	v_exp_f32 v33, v33
	v_exp_f32 v34, v34
	v_smfmac_f32_32x32x32_bf16 v[16:31], v[116:119], v[56:63], v120
	s_mov_b64 exec, s[46:47]
	buffer_load_dwordx4 v[112:115], v122, s[12:15], s44 offen offset:1024 sc1
	buffer_load_dwordx4 v[116:119], v122, s[12:15], s44 offen offset:1536 sc1
	s_mov_b64 exec, -1
	v_exp_f32 v35, v35
	v_exp_f32 v36, v36
	v_exp_f32 v37, v37
	v_mfma_f32_32x32x16_bf16 v[48:63], v[72:75], v[68:71], 0
	s_add_u32 s56, s56, 4096
	s_and_b32 s56, s56, 0x7fff
	s_add_u32 s44, s56, s57
	buffer_load_dwordx4 v[72:75], v127, s[12:15], s44 offen sc1
	v_exp_f32 v38, v38
	v_exp_f32 v39, v39
	v_exp_f32 v40, v40
	v_exp_f32 v41, v41
	s_setprio 2
	v_exp_f32 v42, v42
	v_exp_f32 v43, v43
	v_exp_f32 v44, v44
	v_exp_f32 v45, v45
	v_exp_f32 v46, v46
	v_exp_f32 v47, v47
	v_smfmac_f32_32x32x32_bf16 v[0:15], v[76:79], v[32:39], v120
	v_exp_f32 v48, v48
	v_exp_f32 v49, v49
	v_exp_f32 v50, v50
	v_smfmac_f32_32x32x32_bf16 v[0:15], v[80:83], v[40:47], v120
	v_exp_f32 v51, v51
	v_exp_f32 v52, v52
	v_exp_f32 v53, v53
	v_mfma_f32_32x32x16_bf16 v[32:47], v[84:87], v[64:67], 0
	buffer_load_dwordx4 v[132:135], v121, s[12:15], s39 offen offset:1024 sc1
	s_cmp_lg_u32 s62, 0
	s_cbranch_scc1 .Lk_f7
	buffer_store_dwordx4 v[252:255], v121, s[12:15], s35 offen sc1
.Lk_f7:
	v_exp_f32 v54, v54
	v_exp_f32 v55, v55
	v_exp_f32 v56, v56
	v_exp_f32 v57, v57
	v_exp_f32 v58, v58
	v_exp_f32 v59, v59
	v_exp_f32 v60, v60
	v_exp_f32 v61, v61
	v_exp_f32 v62, v62
	v_exp_f32 v63, v63
	v_smfmac_f32_32x32x32_bf16 v[16:31], v[76:79], v[48:55], v120
	s_setprio 0
	v_exp_f32 v32, v32
	s_setprio 1
	v_exp_f32 v33, v33
	v_exp_f32 v34, v34
	v_smfmac_f32_32x32x32_bf16 v[16:31], v[80:83], v[56:63], v120
	s_mov_b64 exec, s[46:47]
	buffer_load_dwordx4 v[76:79], v122, s[12:15], s44 offen offset:1024 sc1
	buffer_load_dwordx4 v[80:83], v122, s[12:15], s44 offen offset:1536 sc1
	s_mov_b64 exec, -1
	v_exp_f32 v35, v35
	v_exp_f32 v36, v36
	v_exp_f32 v37, v37
	v_mfma_f32_32x32x16_bf16 v[48:63], v[84:87], v[68:71], 0
	s_add_u32 s56, s56, 4096
	s_and_b32 s56, s56, 0x7fff
	s_add_u32 s44, s56, s57
	buffer_load_dwordx4 v[84:87], v127, s[12:15], s44 offen sc1
	v_exp_f32 v38, v38
	v_exp_f32 v39, v39
	v_exp_f32 v40, v40
	v_exp_f32 v41, v41
	s_setprio 2
	v_exp_f32 v42, v42
	v_exp_f32 v43, v43
	v_exp_f32 v44, v44
	v_exp_f32 v45, v45
	v_exp_f32 v46, v46
	v_exp_f32 v47, v47
	v_smfmac_f32_32x32x32_bf16 v[0:15], v[88:91], v[32:39], v120
	v_exp_f32 v48, v48
	v_exp_f32 v49, v49
	v_exp_f32 v50, v50
	v_smfmac_f32_32x32x32_bf16 v[0:15], v[92:95], v[40:47], v120
	v_exp_f32 v51, v51
	v_exp_f32 v52, v52
	v_exp_f32 v53, v53
	v_mfma_f32_32x32x16_bf16 v[32:47], v[96:99], v[64:67], 0
	s_memrealtime s[26:27]
	buffer_load_dwordx4 v[136:139], v121, s[12:15], s39 offen offset:2048 sc1
	buffer_load_dwordx4 v[140:143], v121, s[12:15], s39 offen offset:3072 sc1
	s_add_u32 s39, s39, 4096
	s_cmp_lg_u32 s62, 1
	s_cbranch_scc1 .Lk_f8
	buffer_store_dwordx4 v[252:255], v121, s[12:15], s35 offen sc1
.Lk_f8:
	v_exp_f32 v54, v54
	v_exp_f32 v55, v55
	v_exp_f32 v56, v56
	v_exp_f32 v57, v57
	v_exp_f32 v58, v58
	v_exp_f32 v59, v59
	v_exp_f32 v60, v60
	v_exp_f32 v61, v61
	v_exp_f32 v62, v62
	v_exp_f32 v63, v63
	v_smfmac_f32_32x32x32_bf16 v[16:31], v[88:91], v[48:55], v120
	s_setprio 0
	v_exp_f32 v32, v32
	s_setprio 1
	v_exp_f32 v33, v33
	v_exp_f32 v34, v34
	v_smfmac_f32_32x32x32_bf16 v[16:31], v[92:95], v[56:63], v120
	s_mov_b64 exec, s[46:47]
	buffer_load_dwordx4 v[88:91], v122, s[12:15], s44 offen offset:1024 sc1
	buffer_load_dwordx4 v[92:95], v122, s[12:15], s44 offen offset:1536 sc1
	s_mov_b64 exec, -1
	v_exp_f32 v35, v35
	v_exp_f32 v36, v36
	v_exp_f32 v37, v37
	v_mfma_f32_32x32x16_bf16 v[48:63], v[96:99], v[68:71], 0
	s_add_u32 s56, s56, 4096
	s_and_b32 s56, s56, 0x7fff
	s_add_u32 s44, s56, s57
	buffer_load_dwordx4 v[96:99], v127, s[12:15], s44 offen sc1
	v_exp_f32 v38, v38
	v_exp_f32 v39, v39
	v_exp_f32 v40, v40
	v_exp_f32 v41, v41
	s_setprio 2
	v_exp_f32 v42, v42
	v_exp_f32 v43, v43
	v_exp_f32 v44, v44
	v_exp_f32 v45, v45
	v_exp_f32 v46, v46
	v_exp_f32 v47, v47
	s_waitcnt vmcnt(13)
	v_smfmac_f32_32x32x32_bf16 v[0:15], v[100:103], v[32:39], v120
	v_exp_f32 v48, v48
	v_exp_f32 v49, v49
	v_exp_f32 v50, v50
	v_smfmac_f32_32x32x32_bf16 v[0:15], v[104:107], v[40:47], v120
	v_exp_f32 v51, v51
	v_exp_f32 v52, v52
	v_exp_f32 v53, v53
	v_mfma_f32_32x32x16_bf16 v[32:47], v[108:111], v[64:67], 0
	buffer_load_dwordx4 v[144:147], v121, s[12:15], s39 offen sc1
	v_exp_f32 v54, v54
	v_exp_f32 v55, v55
	v_exp_f32 v56, v56
	v_exp_f32 v57, v57
	v_exp_f32 v58, v58
	v_exp_f32 v59, v59
	v_exp_f32 v60, v60
	v_exp_f32 v61, v61
	v_exp_f32 v62, v62
	v_exp_f32 v63, v63
	v_smfmac_f32_32x32x32_bf16 v[16:31], v[100:103], v[48:55], v120
	s_setprio 0
	v_exp_f32 v32, v32
	s_setprio 1
	v_exp_f32 v33, v33
	v_exp_f32 v34, v34
	v_smfmac_f32_32x32x32_bf16 v[16:31], v[104:107], v[56:63], v120
	s_mov_b64 exec, s[46:47]
	buffer_load_dwordx4 v[100:103], v122, s[12:15], s44 offen offset:1024 sc1
	buffer_load_dwordx4 v[104:107], v122, s[12:15], s44 offen offset:1536 sc1
	s_mov_b64 exec, -1
	v_exp_f32 v35, v35
	v_exp_f32 v36, v36
	v_exp_f32 v37, v37
	v_mfma_f32_32x32x16_bf16 v[48:63], v[108:111], v[68:71], 0
	s_add_u32 s56, s56, 4096
	s_and_b32 s56, s56, 0x7fff
	s_add_u32 s44, s56, s57
	buffer_load_dwordx4 v[108:111], v127, s[12:15], s44 offen sc1
	v_exp_f32 v38, v38
	v_exp_f32 v39, v39
	v_exp_f32 v40, v40
	v_exp_f32 v41, v41
	s_setprio 2
	v_exp_f32 v42, v42
	v_exp_f32 v43, v43
	v_exp_f32 v44, v44
	v_exp_f32 v45, v45
	v_exp_f32 v46, v46
	v_exp_f32 v47, v47
	s_waitcnt vmcnt(13)
	v_smfmac_f32_32x32x32_bf16 v[0:15], v[112:115], v[32:39], v120
	v_exp_f32 v48, v48
	v_exp_f32 v49, v49
	v_exp_f32 v50, v50
	v_smfmac_f32_32x32x32_bf16 v[0:15], v[116:119], v[40:47], v120
	v_exp_f32 v51, v51
	v_exp_f32 v52, v52
	v_exp_f32 v53, v53
	v_mfma_f32_32x32x16_bf16 v[32:47], v[72:75], v[64:67], 0
	buffer_load_dwordx4 v[148:151], v121, s[12:15], s39 offen offset:1024 sc1
	v_exp_f32 v54, v54
	v_exp_f32 v55, v55
	v_exp_f32 v56, v56
	v_exp_f32 v57, v57
	v_exp_f32 v58, v58
	v_exp_f32 v59, v59
	v_exp_f32 v60, v60
	v_exp_f32 v61, v61
	v_exp_f32 v62, v62
	v_exp_f32 v63, v63
	v_smfmac_f32_32x32x32_bf16 v[16:31], v[112:115], v[48:55], v120
	s_setprio 0
	v_exp_f32 v32, v32
	s_setprio 1
	v_exp_f32 v33, v33
	v_exp_f32 v34, v34
	v_smfmac_f32_32x32x32_bf16 v[16:31], v[116:119], v[56:63], v120
	s_mov_b64 exec, s[46:47]
	buffer_load_dwordx4 v[112:115], v122, s[12:15], s44 offen offset:1024 sc1
	buffer_load_dwordx4 v[116:119], v122, s[12:15], s44 offen offset:1536 sc1
	s_mov_b64 exec, -1
	v_exp_f32 v35, v35
	v_exp_f32 v36, v36
	v_exp_f32 v37, v37
	v_mfma_f32_32x32x16_bf16 v[48:63], v[72:75], v[68:71], 0
	s_add_u32 s56, s56, 4096
	s_and_b32 s56, s56, 0x7fff
	s_add_u32 s44, s57, 32768
	s_and_b32 s44, s44, 0x18000
	s_add_u32 s57, s38, s44
	s_add_u32 s44, s56, s57
	buffer_load_dwordx4 v[72:75], v127, s[12:15], s44 offen sc1
	v_exp_f32 v38, v38
	v_exp_f32 v39, v39
	v_exp_f32 v40, v40
	v_exp_f32 v41, v41
	s_setprio 2
	v_exp_f32 v42, v42
	v_exp_f32 v43, v43
	v_exp_f32 v44, v44
	v_exp_f32 v45, v45
	v_exp_f32 v46, v46
	v_exp_f32 v47, v47
	s_waitcnt vmcnt(13)
	v_smfmac_f32_32x32x32_bf16 v[0:15], v[76:79], v[32:39], v120
	v_exp_f32 v48, v48
	v_exp_f32 v49, v49
	v_exp_f32 v50, v50
	v_smfmac_f32_32x32x32_bf16 v[0:15], v[80:83], v[40:47], v120
	v_exp_f32 v51, v51
	v_exp_f32 v52, v52
	v_exp_f32 v53, v53
	v_mfma_f32_32x32x16_bf16 v[32:47], v[84:87], v[64:67], 0
	buffer_load_dwordx4 v[152:155], v121, s[12:15], s39 offen offset:2048 sc1
	buffer_load_dwordx4 v[156:159], v121, s[12:15], s39 offen offset:3072 sc1
	s_add_u32 s39, s39, 4096
	v_exp_f32 v54, v54
	v_exp_f32 v55, v55
	v_exp_f32 v56, v56
	v_exp_f32 v57, v57
	v_exp_f32 v58, v58
	v_exp_f32 v59, v59
	v_exp_f32 v60, v60
	v_exp_f32 v61, v61
	v_exp_f32 v62, v62
	v_exp_f32 v63, v63
	v_smfmac_f32_32x32x32_bf16 v[16:31], v[76:79], v[48:55], v120
	s_setprio 0
	v_exp_f32 v32, v32
	s_setprio 1
	v_exp_f32 v33, v33
	v_exp_f32 v34, v34
	v_smfmac_f32_32x32x32_bf16 v[16:31], v[80:83], v[56:63], v120
	s_mov_b64 exec, s[46:47]
	buffer_load_dwordx4 v[76:79], v122, s[12:15], s44 offen offset:1024 sc1
	buffer_load_dwordx4 v[80:83], v122, s[12:15], s44 offen offset:1536 sc1
	s_mov_b64 exec, -1
	v_exp_f32 v35, v35
	v_exp_f32 v36, v36
	v_exp_f32 v37, v37
	v_mfma_f32_32x32x16_bf16 v[48:63], v[84:87], v[68:71], 0
	s_add_u32 s56, s56, 4096
	s_and_b32 s56, s56, 0x7fff
	s_add_u32 s44, s56, s57
	buffer_load_dwordx4 v[84:87], v127, s[12:15], s44 offen sc1
	v_exp_f32 v38, v38
	v_exp_f32 v39, v39
	v_exp_f32 v40, v40
	v_exp_f32 v41, v41
	s_setprio 2
	v_exp_f32 v42, v42
	v_exp_f32 v43, v43
	v_exp_f32 v44, v44
	v_exp_f32 v45, v45
	v_exp_f32 v46, v46
	v_exp_f32 v47, v47
	s_waitcnt vmcnt(13)
	v_smfmac_f32_32x32x32_bf16 v[0:15], v[88:91], v[32:39], v120
	v_exp_f32 v48, v48
	v_exp_f32 v49, v49
	v_exp_f32 v50, v50
	v_smfmac_f32_32x32x32_bf16 v[0:15], v[92:95], v[40:47], v120
	v_exp_f32 v51, v51
	v_exp_f32 v52, v52
	v_exp_f32 v53, v53
	v_mfma_f32_32x32x16_bf16 v[32:47], v[96:99], v[64:67], 0
	buffer_load_dwordx4 v[160:163], v121, s[12:15], s39 offen sc1
	v_exp_f32 v54, v54
	v_exp_f32 v55, v55
	v_exp_f32 v56, v56
	v_exp_f32 v57, v57
	v_exp_f32 v58, v58
	v_exp_f32 v59, v59
	v_exp_f32 v60, v60
	v_exp_f32 v61, v61
	v_exp_f32 v62, v62
	v_exp_f32 v63, v63
	v_smfmac_f32_32x32x32_bf16 v[16:31], v[88:91], v[48:55], v120
	s_setprio 0
	v_exp_f32 v32, v32
	s_setprio 1
	v_exp_f32 v33, v33
	v_exp_f32 v34, v34
	v_smfmac_f32_32x32x32_bf16 v[16:31], v[92:95], v[56:63], v120
	s_mov_b64 exec, s[46:47]
	buffer_load_dwordx4 v[88:91], v122, s[12:15], s44 offen offset:1024 sc1
	buffer_load_dwordx4 v[92:95], v122, s[12:15], s44 offen offset:1536 sc1
	s_mov_b64 exec, -1
	v_exp_f32 v35, v35
	v_exp_f32 v36, v36
	v_exp_f32 v37, v37
	v_mfma_f32_32x32x16_bf16 v[48:63], v[96:99], v[68:71], 0
	s_add_u32 s56, s56, 4096
	s_and_b32 s56, s56, 0x7fff
	s_add_u32 s44, s56, s57
	buffer_load_dwordx4 v[96:99], v127, s[12:15], s44 offen sc1
	v_exp_f32 v38, v38
	v_exp_f32 v39, v39
	v_exp_f32 v40, v40
	v_exp_f32 v41, v41
	s_setprio 2
	v_exp_f32 v42, v42
	v_exp_f32 v43, v43
	v_exp_f32 v44, v44
	v_exp_f32 v45, v45
	v_exp_f32 v46, v46
	v_exp_f32 v47, v47
	s_waitcnt vmcnt(13)
	v_smfmac_f32_32x32x32_bf16 v[0:15], v[100:103], v[32:39], v120
	v_exp_f32 v48, v48
	v_exp_f32 v49, v49
	v_exp_f32 v50, v50
	v_smfmac_f32_32x32x32_bf16 v[0:15], v[104:107], v[40:47], v120
	v_exp_f32 v51, v51
	v_exp_f32 v52, v52
	v_exp_f32 v53, v53
	v_mfma_f32_32x32x16_bf16 v[32:47], v[108:111], v[64:67], 0
	buffer_store_dwordx4 v[252:255], v121, s[12:15], s24 offen sc1
	s_sub_u32 s24, s24, 0x8000
	buffer_load_dwordx4 v[164:167], v121, s[12:15], s39 offen offset:1024 sc1
	v_exp_f32 v54, v54
	v_exp_f32 v55, v55
	v_exp_f32 v56, v56
	v_exp_f32 v57, v57
	v_exp_f32 v58, v58
	v_exp_f32 v59, v59
	v_exp_f32 v60, v60
	v_exp_f32 v61, v61
	v_exp_f32 v62, v62
	v_exp_f32 v63, v63
	v_smfmac_f32_32x32x32_bf16 v[16:31], v[100:103], v[48:55], v120
	s_setprio 0
	v_exp_f32 v32, v32
	s_setprio 1
	v_exp_f32 v33, v33
	v_exp_f32 v34, v34
	v_smfmac_f32_32x32x32_bf16 v[16:31], v[104:107], v[56:63], v120
	s_mov_b64 exec, s[46:47]
	buffer_load_dwordx4 v[100:103], v122, s[12:15], s44 offen offset:1024 sc1
	buffer_load_dwordx4 v[104:107], v122, s[12:15], s44 offen offset:1536 sc1
	s_mov_b64 exec, -1
	v_exp_f32 v35, v35
	v_exp_f32 v36, v36
	v_exp_f32 v37, v37
	v_mfma_f32_32x32x16_bf16 v[48:63], v[108:111], v[68:71], 0
	s_add_u32 s56, s56, 4096
	s_and_b32 s56, s56, 0x7fff
	s_add_u32 s44, s56, s57
	buffer_load_dwordx4 v[108:111], v127, s[12:15], s44 offen sc1
	v_exp_f32 v38, v38
	v_exp_f32 v39, v39
	v_exp_f32 v40, v40
	v_exp_f32 v41, v41
	s_setprio 2
	v_exp_f32 v42, v42
	v_exp_f32 v43, v43
	v_exp_f32 v44, v44
	v_exp_f32 v45, v45
	v_exp_f32 v46, v46
	v_exp_f32 v47, v47
	s_waitcnt vmcnt(14)
	v_smfmac_f32_32x32x32_bf16 v[0:15], v[112:115], v[32:39], v120
	v_exp_f32 v48, v48
	v_exp_f32 v49, v49
	v_exp_f32 v50, v50
	v_smfmac_f32_32x32x32_bf16 v[0:15], v[116:119], v[40:47], v120
	v_exp_f32 v51, v51
	v_exp_f32 v52, v52
	v_exp_f32 v53, v53
	v_mfma_f32_32x32x16_bf16 v[32:47], v[72:75], v[64:67], 0
	buffer_store_dwordx4 v[252:255], v121, s[12:15], s24 offen sc1
	buffer_load_dwordx4 v[168:171], v121, s[12:15], s39 offen offset:2048 sc1
	buffer_load_dwordx4 v[172:175], v121, s[12:15], s39 offen offset:3072 sc1
	s_add_u32 s39, s39, 4096
	v_exp_f32 v54, v54
	v_exp_f32 v55, v55
	v_exp_f32 v56, v56
	v_exp_f32 v57, v57
	v_exp_f32 v58, v58
	v_exp_f32 v59, v59
	v_exp_f32 v60, v60
	v_exp_f32 v61, v61
	v_exp_f32 v62, v62
	v_exp_f32 v63, v63
	v_smfmac_f32_32x32x32_bf16 v[16:31], v[112:115], v[48:55], v120
	s_setprio 0
	v_exp_f32 v32, v32
	s_setprio 1
	v_exp_f32 v33, v33
	v_exp_f32 v34, v34
	v_smfmac_f32_32x32x32_bf16 v[16:31], v[116:119], v[56:63], v120
	s_mov_b64 exec, s[46:47]
	buffer_load_dwordx4 v[112:115], v122, s[12:15], s44 offen offset:1024 sc1
	buffer_load_dwordx4 v[116:119], v122, s[12:15], s44 offen offset:1536 sc1
	s_mov_b64 exec, -1
	v_exp_f32 v35, v35
	v_exp_f32 v36, v36
	v_exp_f32 v37, v37
	v_mfma_f32_32x32x16_bf16 v[48:63], v[72:75], v[68:71], 0
	s_add_u32 s56, s56, 4096
	s_and_b32 s56, s56, 0x7fff
	s_add_u32 s44, s56, s57
	buffer_load_dwordx4 v[72:75], v127, s[12:15], s44 offen sc1
	v_exp_f32 v38, v38
	v_exp_f32 v39, v39
	v_exp_f32 v40, v40
	v_exp_f32 v41, v41
	s_setprio 2
	v_exp_f32 v42, v42
	v_exp_f32 v43, v43
	v_exp_f32 v44, v44
	v_exp_f32 v45, v45
	v_exp_f32 v46, v46
	v_exp_f32 v47, v47
	s_waitcnt vmcnt(15)
	v_smfmac_f32_32x32x32_bf16 v[0:15], v[76:79], v[32:39], v120
	v_exp_f32 v48, v48
	v_exp_f32 v49, v49
	v_exp_f32 v50, v50
	v_smfmac_f32_32x32x32_bf16 v[0:15], v[80:83], v[40:47], v120
	v_exp_f32 v51, v51
	v_exp_f32 v52, v52
	v_exp_f32 v53, v53
	v_mfma_f32_32x32x16_bf16 v[32:47], v[84:87], v[64:67], 0
	buffer_load_dwordx4 v[176:179], v121, s[12:15], s39 offen sc1
	s_cmp_lg_u32 s62, 0
	s_cbranch_scc1 .Lk_f9
	buffer_store_dwordx4 v[252:255], v121, s[12:15], s35 offen offset:1024 sc1
.Lk_f9:
	v_exp_f32 v54, v54
	v_exp_f32 v55, v55
	v_exp_f32 v56, v56
	v_exp_f32 v57, v57
	v_exp_f32 v58, v58
	v_exp_f32 v59, v59
	v_exp_f32 v60, v60
	v_exp_f32 v61, v61
	v_exp_f32 v62, v62
	v_exp_f32 v63, v63
	v_smfmac_f32_32x32x32_bf16 v[16:31], v[76:79], v[48:55], v120
	s_setprio 0
	v_exp_f32 v32, v32
	s_setprio 1
	v_exp_f32 v33, v33
	v_exp_f32 v34, v34
	v_smfmac_f32_32x32x32_bf16 v[16:31], v[80:83], v[56:63], v120
	s_mov_b64 exec, s[46:47]
	buffer_load_dwordx4 v[76:79], v122, s[12:15], s44 offen offset:1024 sc1
	buffer_load_dwordx4 v[80:83], v122, s[12:15], s44 offen offset:1536 sc1
	s_mov_b64 exec, -1
	v_exp_f32 v35, v35
	v_exp_f32 v36, v36
	v_exp_f32 v37, v37
	v_mfma_f32_32x32x16_bf16 v[48:63], v[84:87], v[68:71], 0
	s_add_u32 s56, s56, 4096
	s_and_b32 s56, s56, 0x7fff
	s_add_u32 s44, s56, s57
	buffer_load_dwordx4 v[84:87], v127, s[12:15], s44 offen sc1
	v_exp_f32 v38, v38
	v_exp_f32 v39, v39
	v_exp_f32 v40, v40
	v_exp_f32 v41, v41
	s_setprio 2
	v_exp_f32 v42, v42
	v_exp_f32 v43, v43
	v_exp_f32 v44, v44
	v_exp_f32 v45, v45
	v_exp_f32 v46, v46
	v_exp_f32 v47, v47
	s_waitcnt vmcnt(15)
	v_smfmac_f32_32x32x32_bf16 v[0:15], v[88:91], v[32:39], v120
	v_exp_f32 v48, v48
	v_exp_f32 v49, v49
	v_exp_f32 v50, v50
	v_smfmac_f32_32x32x32_bf16 v[0:15], v[92:95], v[40:47], v120
	v_exp_f32 v51, v51
	v_exp_f32 v52, v52
	v_exp_f32 v53, v53
	v_mfma_f32_32x32x16_bf16 v[32:47], v[96:99], v[64:67], 0
	buffer_load_dwordx4 v[180:183], v121, s[12:15], s39 offen offset:1024 sc1
	s_cmp_lg_u32 s62, 1
	s_cbranch_scc1 .Lk_f10
	buffer_store_dwordx4 v[252:255], v121, s[12:15], s35 offen offset:1024 sc1
.Lk_f10:
	v_exp_f32 v54, v54
	v_exp_f32 v55, v55
	v_exp_f32 v56, v56
	v_exp_f32 v57, v57
	v_exp_f32 v58, v58
	v_exp_f32 v59, v59
	v_exp_f32 v60, v60
	v_exp_f32 v61, v61
	v_exp_f32 v62, v62
	v_exp_f32 v63, v63
	v_smfmac_f32_32x32x32_bf16 v[16:31], v[88:91], v[48:55], v120
	s_setprio 0
	v_exp_f32 v32, v32
	s_setprio 1
	v_exp_f32 v33, v33
	v_exp_f32 v34, v34
	v_smfmac_f32_32x32x32_bf16 v[16:31], v[92:95], v[56:63], v120
	s_mov_b64 exec, s[46:47]
	buffer_load_dwordx4 v[88:91], v122, s[12:15], s44 offen offset:1024 sc1
	buffer_load_dwordx4 v[92:95], v122, s[12:15], s44 offen offset:1536 sc1
	s_mov_b64 exec, -1
	v_exp_f32 v35, v35
	v_exp_f32 v36, v36
	v_exp_f32 v37, v37
	v_mfma_f32_32x32x16_bf16 v[48:63], v[96:99], v[68:71], 0
	s_add_u32 s56, s56, 4096
	s_and_b32 s56, s56, 0x7fff
	s_add_u32 s44, s56, s57
	buffer_load_dwordx4 v[96:99], v127, s[12:15], s44 offen sc1
	v_exp_f32 v38, v38
	v_exp_f32 v39, v39
	v_exp_f32 v40, v40
	v_exp_f32 v41, v41
	s_setprio 2
	v_exp_f32 v42, v42
	v_exp_f32 v43, v43
	v_exp_f32 v44, v44
	v_exp_f32 v45, v45
	v_exp_f32 v46, v46
	v_exp_f32 v47, v47
	s_waitcnt vmcnt(14)
	v_smfmac_f32_32x32x32_bf16 v[0:15], v[100:103], v[32:39], v120
	v_exp_f32 v48, v48
	v_exp_f32 v49, v49
	v_exp_f32 v50, v50
	v_smfmac_f32_32x32x32_bf16 v[0:15], v[104:107], v[40:47], v120
	v_exp_f32 v51, v51
	v_exp_f32 v52, v52
	v_exp_f32 v53, v53
	v_mfma_f32_32x32x16_bf16 v[32:47], v[108:111], v[64:67], 0
	s_memrealtime s[24:25]
	buffer_load_dwordx4 v[184:187], v121, s[12:15], s39 offen offset:2048 sc1
	buffer_load_dwordx4 v[188:191], v121, s[12:15], s39 offen offset:3072 sc1
	s_add_u32 s39, s39, 4096
	v_exp_f32 v54, v54
	v_exp_f32 v55, v55
	v_exp_f32 v56, v56
	v_exp_f32 v57, v57
	v_exp_f32 v58, v58
	v_exp_f32 v59, v59
	v_exp_f32 v60, v60
	v_exp_f32 v61, v61
	v_exp_f32 v62, v62
	v_exp_f32 v63, v63
	v_smfmac_f32_32x32x32_bf16 v[16:31], v[100:103], v[48:55], v120
	s_setprio 0
	v_exp_f32 v32, v32
	s_setprio 1
	v_exp_f32 v33, v33
	v_exp_f32 v34, v34
	v_smfmac_f32_32x32x32_bf16 v[16:31], v[104:107], v[56:63], v120
	s_mov_b64 exec, s[46:47]
	buffer_load_dwordx4 v[100:103], v122, s[12:15], s44 offen offset:1024 sc1
	buffer_load_dwordx4 v[104:107], v122, s[12:15], s44 offen offset:1536 sc1
	s_mov_b64 exec, -1
	v_exp_f32 v35, v35
	v_exp_f32 v36, v36
	v_exp_f32 v37, v37
	v_mfma_f32_32x32x16_bf16 v[48:63], v[108:111], v[68:71], 0
	s_add_u32 s56, s56, 4096
	s_and_b32 s56, s56, 0x7fff
	s_add_u32 s44, s56, s57
	buffer_load_dwordx4 v[108:111], v127, s[12:15], s44 offen sc1
	v_exp_f32 v38, v38
	v_exp_f32 v39, v39
	v_exp_f32 v40, v40
	v_exp_f32 v41, v41
	s_setprio 2
	v_exp_f32 v42, v42
	v_exp_f32 v43, v43
	v_exp_f32 v44, v44
	v_exp_f32 v45, v45
	v_exp_f32 v46, v46
	v_exp_f32 v47, v47
	s_waitcnt vmcnt(13)
	v_smfmac_f32_32x32x32_bf16 v[0:15], v[112:115], v[32:39], v120
	v_exp_f32 v48, v48
	v_exp_f32 v49, v49
	v_exp_f32 v50, v50
	v_smfmac_f32_32x32x32_bf16 v[0:15], v[116:119], v[40:47], v120
	v_exp_f32 v51, v51
	v_exp_f32 v52, v52
	v_exp_f32 v53, v53
	v_mfma_f32_32x32x16_bf16 v[32:47], v[72:75], v[64:67], 0
	s_waitcnt lgkmcnt(0)
	s_sub_u32 s26, s24, s26
	s_lshr_b32 s26, s26, 1
	s_add_u32 s24, s24, s26
	s_or_b32 s24, s24, 1
	v_mov_b32 v248, s24
	s_mov_b64 exec, 1
	buffer_store_dword v248, off, s[12:15], s48 sc1
	s_mov_b64 exec, -1
	buffer_load_dwordx4 v[192:195], v121, s[12:15], s39 offen sc1
	v_exp_f32 v54, v54
	v_exp_f32 v55, v55
	v_exp_f32 v56, v56
	v_exp_f32 v57, v57
	v_exp_f32 v58, v58
	v_exp_f32 v59, v59
	v_exp_f32 v60, v60
	v_exp_f32 v61, v61
	v_exp_f32 v62, v62
	v_exp_f32 v63, v63
	v_smfmac_f32_32x32x32_bf16 v[16:31], v[112:115], v[48:55], v120
	s_setprio 0
	v_exp_f32 v32, v32
	s_setprio 1
	v_exp_f32 v33, v33
	v_exp_f32 v34, v34
	v_smfmac_f32_32x32x32_bf16 v[16:31], v[116:119], v[56:63], v120
	s_mov_b64 exec, s[46:47]
	buffer_load_dwordx4 v[112:115], v122, s[12:15], s44 offen offset:1024 sc1
	buffer_load_dwordx4 v[116:119], v122, s[12:15], s44 offen offset:1536 sc1
	s_mov_b64 exec, -1
	v_exp_f32 v35, v35
	v_exp_f32 v36, v36
	v_exp_f32 v37, v37
	v_mfma_f32_32x32x16_bf16 v[48:63], v[72:75], v[68:71], 0
	s_add_u32 s56, s56, 4096
	s_and_b32 s56, s56, 0x7fff
	s_add_u32 s44, s57, 32768
	s_and_b32 s44, s44, 0x18000
	s_add_u32 s57, s38, s44
	s_add_u32 s44, s56, s57
	buffer_load_dwordx4 v[72:75], v127, s[12:15], s44 offen sc1
	v_exp_f32 v38, v38
	v_exp_f32 v39, v39
	v_exp_f32 v40, v40
	v_exp_f32 v41, v41
	s_setprio 2
	v_exp_f32 v42, v42
	v_exp_f32 v43, v43
	v_exp_f32 v44, v44
	v_exp_f32 v45, v45
	v_exp_f32 v46, v46
	v_exp_f32 v47, v47
	s_waitcnt vmcnt(14)
	v_smfmac_f32_32x32x32_bf16 v[0:15], v[76:79], v[32:39], v120
	v_exp_f32 v48, v48
	v_exp_f32 v49, v49
	v_exp_f32 v50, v50
	v_smfmac_f32_32x32x32_bf16 v[0:15], v[80:83], v[40:47], v120
	v_exp_f32 v51, v51
	v_exp_f32 v52, v52
	v_exp_f32 v53, v53
	v_mfma_f32_32x32x16_bf16 v[32:47], v[84:87], v[64:67], 0
	buffer_load_dwordx4 v[196:199], v121, s[12:15], s39 offen offset:1024 sc1
	v_exp_f32 v54, v54
	v_exp_f32 v55, v55
	v_exp_f32 v56, v56
	v_exp_f32 v57, v57
	v_exp_f32 v58, v58
	v_exp_f32 v59, v59
	v_exp_f32 v60, v60
	v_exp_f32 v61, v61
	v_exp_f32 v62, v62
	v_exp_f32 v63, v63
	v_smfmac_f32_32x32x32_bf16 v[16:31], v[76:79], v[48:55], v120
	s_setprio 0
	v_exp_f32 v32, v32
	s_setprio 1
	v_exp_f32 v33, v33
	v_exp_f32 v34, v34
	v_smfmac_f32_32x32x32_bf16 v[16:31], v[80:83], v[56:63], v120
	s_mov_b64 exec, s[46:47]
	buffer_load_dwordx4 v[76:79], v122, s[12:15], s44 offen offset:1024 sc1
	buffer_load_dwordx4 v[80:83], v122, s[12:15], s44 offen offset:1536 sc1
	s_mov_b64 exec, -1
	v_exp_f32 v35, v35
	v_exp_f32 v36, v36
	v_exp_f32 v37, v37
	v_mfma_f32_32x32x16_bf16 v[48:63], v[84:87], v[68:71], 0
	s_add_u32 s56, s56, 4096
	s_and_b32 s56, s56, 0x7fff
	s_add_u32 s44, s56, s57
	buffer_load_dwordx4 v[84:87], v127, s[12:15], s44 offen sc1
	v_exp_f32 v38, v38
	v_exp_f32 v39, v39
	v_exp_f32 v40, v40
	v_exp_f32 v41, v41
	s_setprio 2
	v_exp_f32 v42, v42
	v_exp_f32 v43, v43
	v_exp_f32 v44, v44
	v_exp_f32 v45, v45
	v_exp_f32 v46, v46
	v_exp_f32 v47, v47
	s_waitcnt vmcnt(14)
	v_smfmac_f32_32x32x32_bf16 v[0:15], v[88:91], v[32:39], v120
	v_exp_f32 v48, v48
	v_exp_f32 v49, v49
	v_exp_f32 v50, v50
	v_smfmac_f32_32x32x32_bf16 v[0:15], v[92:95], v[40:47], v120
	v_exp_f32 v51, v51
	v_exp_f32 v52, v52
	v_exp_f32 v53, v53
	v_mfma_f32_32x32x16_bf16 v[32:47], v[96:99], v[64:67], 0
	buffer_load_dwordx4 v[200:203], v121, s[12:15], s39 offen offset:2048 sc1
	buffer_load_dwordx4 v[204:207], v121, s[12:15], s39 offen offset:3072 sc1
	s_add_u32 s39, s39, 4096
	v_exp_f32 v54, v54
	v_exp_f32 v55, v55
	v_exp_f32 v56, v56
	v_exp_f32 v57, v57
	v_exp_f32 v58, v58
	v_exp_f32 v59, v59
	v_exp_f32 v60, v60
	v_exp_f32 v61, v61
	v_exp_f32 v62, v62
	v_exp_f32 v63, v63
	v_smfmac_f32_32x32x32_bf16 v[16:31], v[88:91], v[48:55], v120
	s_setprio 0
	v_exp_f32 v32, v32
	s_setprio 1
	v_exp_f32 v33, v33
	v_exp_f32 v34, v34
	v_smfmac_f32_32x32x32_bf16 v[16:31], v[92:95], v[56:63], v120
	s_mov_b64 exec, s[46:47]
	buffer_load_dwordx4 v[88:91], v122, s[12:15], s44 offen offset:1024 sc1
	buffer_load_dwordx4 v[92:95], v122, s[12:15], s44 offen offset:1536 sc1
	s_mov_b64 exec, -1
	v_exp_f32 v35, v35
	v_exp_f32 v36, v36
	v_exp_f32 v37, v37
	v_mfma_f32_32x32x16_bf16 v[48:63], v[96:99], v[68:71], 0
	s_add_u32 s56, s56, 4096
	s_and_b32 s56, s56, 0x7fff
	s_add_u32 s44, s56, s57
	buffer_load_dwordx4 v[96:99], v127, s[12:15], s44 offen sc1
	v_exp_f32 v38, v38
	v_exp_f32 v39, v39
	v_exp_f32 v40, v40
	v_exp_f32 v41, v41
	s_setprio 2
	v_exp_f32 v42, v42
	v_exp_f32 v43, v43
	v_exp_f32 v44, v44
	v_exp_f32 v45, v45
	v_exp_f32 v46, v46
	v_exp_f32 v47, v47
	s_waitcnt vmcnt(14)
	v_smfmac_f32_32x32x32_bf16 v[0:15], v[100:103], v[32:39], v120
	v_exp_f32 v48, v48
	v_exp_f32 v49, v49
	v_exp_f32 v50, v50
	v_smfmac_f32_32x32x32_bf16 v[0:15], v[104:107], v[40:47], v120
	v_exp_f32 v51, v51
	v_exp_f32 v52, v52
	v_exp_f32 v53, v53
	v_mfma_f32_32x32x16_bf16 v[32:47], v[108:111], v[64:67], 0
	buffer_load_dwordx4 v[208:211], v121, s[12:15], s39 offen sc1
	v_exp_f32 v54, v54
	v_exp_f32 v55, v55
	v_exp_f32 v56, v56
	v_exp_f32 v57, v57
	v_exp_f32 v58, v58
	v_exp_f32 v59, v59
	v_exp_f32 v60, v60
	v_exp_f32 v61, v61
	v_exp_f32 v62, v62
	v_exp_f32 v63, v63
	v_smfmac_f32_32x32x32_bf16 v[16:31], v[100:103], v[48:55], v120
	s_setprio 0
	v_exp_f32 v32, v32
	s_setprio 1
	v_exp_f32 v33, v33
	v_exp_f32 v34, v34
	v_smfmac_f32_32x32x32_bf16 v[16:31], v[104:107], v[56:63], v120
	s_mov_b64 exec, s[46:47]
	buffer_load_dwordx4 v[100:103], v122, s[12:15], s44 offen offset:1024 sc1
	buffer_load_dwordx4 v[104:107], v122, s[12:15], s44 offen offset:1536 sc1
	s_mov_b64 exec, -1
	v_exp_f32 v35, v35
	v_exp_f32 v36, v36
	v_exp_f32 v37, v37
	v_mfma_f32_32x32x16_bf16 v[48:63], v[108:111], v[68:71], 0
	s_add_u32 s56, s56, 4096
	s_and_b32 s56, s56, 0x7fff
	s_add_u32 s44, s56, s57
	buffer_load_dwordx4 v[108:111], v127, s[12:15], s44 offen sc1
	v_exp_f32 v38, v38
	v_exp_f32 v39, v39
	v_exp_f32 v40, v40
	v_exp_f32 v41, v41
	s_setprio 2
	v_exp_f32 v42, v42
	v_exp_f32 v43, v43
	v_exp_f32 v44, v44
	v_exp_f32 v45, v45
	v_exp_f32 v46, v46
	v_exp_f32 v47, v47
	s_waitcnt vmcnt(13)
	v_smfmac_f32_32x32x32_bf16 v[0:15], v[112:115], v[32:39], v120
	v_exp_f32 v48, v48
	v_exp_f32 v49, v49
	v_exp_f32 v50, v50
	v_smfmac_f32_32x32x32_bf16 v[0:15], v[116:119], v[40:47], v120
	v_exp_f32 v51, v51
	v_exp_f32 v52, v52
	v_exp_f32 v53, v53
	v_mfma_f32_32x32x16_bf16 v[32:47], v[72:75], v[64:67], 0
	buffer_load_dwordx4 v[212:215], v121, s[12:15], s39 offen offset:1024 sc1
	s_cmp_lg_u32 s62, 0
	s_cbranch_scc1 .Lk_f11
	buffer_store_dwordx4 v[252:255], v121, s[12:15], s45 offen sc1
.Lk_f11:
	v_exp_f32 v54, v54
	v_exp_f32 v55, v55
	v_exp_f32 v56, v56
	v_exp_f32 v57, v57
	v_exp_f32 v58, v58
	v_exp_f32 v59, v59
	v_exp_f32 v60, v60
	v_exp_f32 v61, v61
	v_exp_f32 v62, v62
	v_exp_f32 v63, v63
	v_smfmac_f32_32x32x32_bf16 v[16:31], v[112:115], v[48:55], v120
	s_setprio 0
	v_exp_f32 v32, v32
	s_setprio 1
	v_exp_f32 v33, v33
	v_exp_f32 v34, v34
	v_smfmac_f32_32x32x32_bf16 v[16:31], v[116:119], v[56:63], v120
	s_mov_b64 exec, s[46:47]
	buffer_load_dwordx4 v[112:115], v122, s[12:15], s44 offen offset:1024 sc1
	buffer_load_dwordx4 v[116:119], v122, s[12:15], s44 offen offset:1536 sc1
	s_mov_b64 exec, -1
	v_exp_f32 v35, v35
	v_exp_f32 v36, v36
	v_exp_f32 v37, v37
	v_mfma_f32_32x32x16_bf16 v[48:63], v[72:75], v[68:71], 0
	s_add_u32 s56, s56, 4096
	s_and_b32 s56, s56, 0x7fff
	s_add_u32 s44, s56, s57
	buffer_load_dwordx4 v[72:75], v127, s[12:15], s44 offen sc1
	v_exp_f32 v38, v38
	v_exp_f32 v39, v39
	v_exp_f32 v40, v40
	v_exp_f32 v41, v41
	s_setprio 2
	v_exp_f32 v42, v42
	v_exp_f32 v43, v43
	v_exp_f32 v44, v44
	v_exp_f32 v45, v45
	v_exp_f32 v46, v46
	v_exp_f32 v47, v47
	s_waitcnt vmcnt(13)
	v_smfmac_f32_32x32x32_bf16 v[0:15], v[76:79], v[32:39], v120
	v_exp_f32 v48, v48
	v_exp_f32 v49, v49
	v_exp_f32 v50, v50
	v_smfmac_f32_32x32x32_bf16 v[0:15], v[80:83], v[40:47], v120
	v_exp_f32 v51, v51
	v_exp_f32 v52, v52
	v_exp_f32 v53, v53
	v_mfma_f32_32x32x16_bf16 v[32:47], v[84:87], v[64:67], 0
	buffer_load_dwordx4 v[216:219], v121, s[12:15], s39 offen offset:2048 sc1
	buffer_load_dwordx4 v[220:223], v121, s[12:15], s39 offen offset:3072 sc1
	s_add_u32 s39, s39, 4096
	s_cmp_lg_u32 s62, 1
	s_cbranch_scc1 .Lk_f12
	buffer_store_dwordx4 v[252:255], v121, s[12:15], s45 offen sc1
.Lk_f12:
	v_exp_f32 v54, v54
	v_exp_f32 v55, v55
	v_exp_f32 v56, v56
	v_exp_f32 v57, v57
	v_exp_f32 v58, v58
	v_exp_f32 v59, v59
	v_exp_f32 v60, v60
	v_exp_f32 v61, v61
	v_exp_f32 v62, v62
	v_exp_f32 v63, v63
	v_smfmac_f32_32x32x32_bf16 v[16:31], v[76:79], v[48:55], v120
	s_setprio 0
	v_exp_f32 v32, v32
	s_setprio 1
	v_exp_f32 v33, v33
	v_exp_f32 v34, v34
	v_smfmac_f32_32x32x32_bf16 v[16:31], v[80:83], v[56:63], v120
	s_mov_b64 exec, s[46:47]
	buffer_load_dwordx4 v[76:79], v122, s[12:15], s44 offen offset:1024 sc1
	buffer_load_dwordx4 v[80:83], v122, s[12:15], s44 offen offset:1536 sc1
	s_mov_b64 exec, -1
	v_exp_f32 v35, v35
	v_exp_f32 v36, v36
	v_exp_f32 v37, v37
	v_mfma_f32_32x32x16_bf16 v[48:63], v[84:87], v[68:71], 0
	s_add_u32 s56, s56, 4096
	s_and_b32 s56, s56, 0x7fff
	s_add_u32 s44, s56, s57
	buffer_load_dwordx4 v[84:87], v127, s[12:15], s44 offen sc1
	v_exp_f32 v38, v38
	v_exp_f32 v39, v39
	v_exp_f32 v40, v40
	v_exp_f32 v41, v41
	s_setprio 2
	v_exp_f32 v42, v42
	v_exp_f32 v43, v43
	v_exp_f32 v44, v44
	v_exp_f32 v45, v45
	v_exp_f32 v46, v46
	v_exp_f32 v47, v47
	s_waitcnt vmcnt(13)
	v_smfmac_f32_32x32x32_bf16 v[0:15], v[88:91], v[32:39], v120
	v_exp_f32 v48, v48
	v_exp_f32 v49, v49
	v_exp_f32 v50, v50
	v_smfmac_f32_32x32x32_bf16 v[0:15], v[92:95], v[40:47], v120
	v_exp_f32 v51, v51
	v_exp_f32 v52, v52
	v_exp_f32 v53, v53
	v_mfma_f32_32x32x16_bf16 v[32:47], v[96:99], v[64:67], 0
	buffer_load_dwordx4 v[224:227], v121, s[12:15], s39 offen sc1
	s_cmp_lg_u32 s62, 0
	s_cbranch_scc1 .Lk_f13
	buffer_store_dwordx4 v[252:255], v121, s[12:15], s45 offen offset:1024 sc1
.Lk_f13:
	v_exp_f32 v54, v54
	v_exp_f32 v55, v55
	v_exp_f32 v56, v56
	v_exp_f32 v57, v57
	v_exp_f32 v58, v58
	v_exp_f32 v59, v59
	v_exp_f32 v60, v60
	v_exp_f32 v61, v61
	v_exp_f32 v62, v62
	v_exp_f32 v63, v63
	v_smfmac_f32_32x32x32_bf16 v[16:31], v[88:91], v[48:55], v120
	s_setprio 0
	v_exp_f32 v32, v32
	s_setprio 1
	v_exp_f32 v33, v33
	v_exp_f32 v34, v34
	v_smfmac_f32_32x32x32_bf16 v[16:31], v[92:95], v[56:63], v120
	s_mov_b64 exec, s[46:47]
	buffer_load_dwordx4 v[88:91], v122, s[12:15], s44 offen offset:1024 sc1
	buffer_load_dwordx4 v[92:95], v122, s[12:15], s44 offen offset:1536 sc1
	s_mov_b64 exec, -1
	v_exp_f32 v35, v35
	v_exp_f32 v36, v36
	v_exp_f32 v37, v37
	v_mfma_f32_32x32x16_bf16 v[48:63], v[96:99], v[68:71], 0
	s_add_u32 s56, s56, 4096
	s_and_b32 s56, s56, 0x7fff
	s_add_u32 s44, s56, s57
	buffer_load_dwordx4 v[96:99], v127, s[12:15], s44 offen sc1
	v_exp_f32 v38, v38
	v_exp_f32 v39, v39
	v_exp_f32 v40, v40
	v_exp_f32 v41, v41
	s_setprio 2
	v_exp_f32 v42, v42
	v_exp_f32 v43, v43
	v_exp_f32 v44, v44
	v_exp_f32 v45, v45
	v_exp_f32 v46, v46
	v_exp_f32 v47, v47
	s_waitcnt vmcnt(13)
	v_smfmac_f32_32x32x32_bf16 v[0:15], v[100:103], v[32:39], v120
	v_exp_f32 v48, v48
	v_exp_f32 v49, v49
	v_exp_f32 v50, v50
	v_smfmac_f32_32x32x32_bf16 v[0:15], v[104:107], v[40:47], v120
	v_exp_f32 v51, v51
	v_exp_f32 v52, v52
	v_exp_f32 v53, v53
	v_mfma_f32_32x32x16_bf16 v[32:47], v[108:111], v[64:67], 0
	buffer_load_dwordx4 v[228:231], v121, s[12:15], s39 offen offset:1024 sc1
	s_cmp_lg_u32 s62, 1
	s_cbranch_scc1 .Lk_f14
	buffer_store_dwordx4 v[252:255], v121, s[12:15], s45 offen offset:1024 sc1
.Lk_f14:
	v_exp_f32 v54, v54
	v_exp_f32 v55, v55
	v_exp_f32 v56, v56
	v_exp_f32 v57, v57
	v_exp_f32 v58, v58
	v_exp_f32 v59, v59
	v_exp_f32 v60, v60
	v_exp_f32 v61, v61
	v_exp_f32 v62, v62
	v_exp_f32 v63, v63
	v_smfmac_f32_32x32x32_bf16 v[16:31], v[100:103], v[48:55], v120
	s_setprio 0
	v_exp_f32 v32, v32
	s_setprio 1
	v_exp_f32 v33, v33
	v_exp_f32 v34, v34
	v_smfmac_f32_32x32x32_bf16 v[16:31], v[104:107], v[56:63], v120
	s_mov_b64 exec, s[46:47]
	buffer_load_dwordx4 v[100:103], v122, s[12:15], s44 offen offset:1024 sc1
	buffer_load_dwordx4 v[104:107], v122, s[12:15], s44 offen offset:1536 sc1
	s_mov_b64 exec, -1
	v_exp_f32 v35, v35
	v_exp_f32 v36, v36
	v_exp_f32 v37, v37
	v_mfma_f32_32x32x16_bf16 v[48:63], v[108:111], v[68:71], 0
	s_add_u32 s56, s56, 4096
	s_and_b32 s56, s56, 0x7fff
	s_add_u32 s44, s56, s57
	buffer_load_dwordx4 v[108:111], v127, s[12:15], s44 offen sc1
	v_exp_f32 v38, v38
	v_exp_f32 v39, v39
	v_exp_f32 v40, v40
	v_exp_f32 v41, v41
	s_setprio 2
	v_exp_f32 v42, v42
	v_exp_f32 v43, v43
	v_exp_f32 v44, v44
	v_exp_f32 v45, v45
	v_exp_f32 v46, v46
	v_exp_f32 v47, v47
	s_waitcnt vmcnt(13)
	v_smfmac_f32_32x32x32_bf16 v[0:15], v[112:115], v[32:39], v120
	v_exp_f32 v48, v48
	v_exp_f32 v49, v49
	v_exp_f32 v50, v50
	v_smfmac_f32_32x32x32_bf16 v[0:15], v[116:119], v[40:47], v120
	v_exp_f32 v51, v51
	v_exp_f32 v52, v52
	v_exp_f32 v53, v53
	v_mfma_f32_32x32x16_bf16 v[32:47], v[72:75], v[64:67], 0
	buffer_load_dwordx4 v[232:235], v121, s[12:15], s39 offen offset:2048 sc1
	buffer_load_dwordx4 v[236:239], v121, s[12:15], s39 offen offset:3072 sc1
	s_add_u32 s39, s39, 4096
	v_exp_f32 v54, v54
	v_exp_f32 v55, v55
	v_exp_f32 v56, v56
	v_exp_f32 v57, v57
	v_exp_f32 v58, v58
	v_exp_f32 v59, v59
	v_exp_f32 v60, v60
	v_exp_f32 v61, v61
	v_exp_f32 v62, v62
	v_exp_f32 v63, v63
	v_smfmac_f32_32x32x32_bf16 v[16:31], v[112:115], v[48:55], v120
	s_setprio 0
	v_exp_f32 v32, v32
	s_setprio 1
	v_exp_f32 v33, v33
	v_exp_f32 v34, v34
	v_smfmac_f32_32x32x32_bf16 v[16:31], v[116:119], v[56:63], v120
	s_mov_b64 exec, s[46:47]
	buffer_load_dwordx4 v[112:115], v122, s[12:15], s44 offen offset:1024 sc1
	buffer_load_dwordx4 v[116:119], v122, s[12:15], s44 offen offset:1536 sc1
	s_mov_b64 exec, -1
	v_exp_f32 v35, v35
	v_exp_f32 v36, v36
	v_exp_f32 v37, v37
	v_mfma_f32_32x32x16_bf16 v[48:63], v[72:75], v[68:71], 0
	v_exp_f32 v38, v38
	v_exp_f32 v39, v39
	v_exp_f32 v40, v40
	v_exp_f32 v41, v41
	s_setprio 2
	v_exp_f32 v42, v42
	v_exp_f32 v43, v43
	v_exp_f32 v44, v44
	v_exp_f32 v45, v45
	v_exp_f32 v46, v46
	v_exp_f32 v47, v47
	s_waitcnt vmcnt(12)
	v_smfmac_f32_32x32x32_bf16 v[0:15], v[76:79], v[32:39], v120
	v_exp_f32 v48, v48
	v_exp_f32 v49, v49
	v_exp_f32 v50, v50
	v_smfmac_f32_32x32x32_bf16 v[0:15], v[80:83], v[40:47], v120
	v_exp_f32 v51, v51
	v_exp_f32 v52, v52
	v_exp_f32 v53, v53
	v_mfma_f32_32x32x16_bf16 v[32:47], v[84:87], v[64:67], 0
	buffer_load_dwordx4 v[240:243], v121, s[12:15], s39 offen sc1
	v_exp_f32 v54, v54
	v_exp_f32 v55, v55
	v_exp_f32 v56, v56
	v_exp_f32 v57, v57
	v_exp_f32 v58, v58
	v_exp_f32 v59, v59
	v_exp_f32 v60, v60
	v_exp_f32 v61, v61
	v_exp_f32 v62, v62
	v_exp_f32 v63, v63
	v_smfmac_f32_32x32x32_bf16 v[16:31], v[76:79], v[48:55], v120
	s_setprio 0
	v_exp_f32 v32, v32
	s_setprio 1
	v_exp_f32 v33, v33
	v_exp_f32 v34, v34
	v_smfmac_f32_32x32x32_bf16 v[16:31], v[80:83], v[56:63], v120
	v_exp_f32 v35, v35
	v_exp_f32 v36, v36
	v_exp_f32 v37, v37
	v_mfma_f32_32x32x16_bf16 v[48:63], v[84:87], v[68:71], 0
	v_exp_f32 v38, v38
	v_exp_f32 v39, v39
	v_exp_f32 v40, v40
	v_exp_f32 v41, v41
	s_setprio 2
	v_exp_f32 v42, v42
	v_exp_f32 v43, v43
	v_exp_f32 v44, v44
	v_exp_f32 v45, v45
	v_exp_f32 v46, v46
	v_exp_f32 v47, v47
	s_waitcnt vmcnt(9)
	v_smfmac_f32_32x32x32_bf16 v[0:15], v[88:91], v[32:39], v120
	v_exp_f32 v48, v48
	v_exp_f32 v49, v49
	v_exp_f32 v50, v50
	v_smfmac_f32_32x32x32_bf16 v[0:15], v[92:95], v[40:47], v120
	v_exp_f32 v51, v51
	v_exp_f32 v52, v52
	v_exp_f32 v53, v53
	v_mfma_f32_32x32x16_bf16 v[32:47], v[96:99], v[64:67], 0
	buffer_load_dwordx4 v[244:247], v121, s[12:15], s39 offen offset:1024 sc1
	buffer_load_dword v125, v124, s[12:15], s51 offen sc1
	v_exp_f32 v54, v54
	v_exp_f32 v55, v55
	v_exp_f32 v56, v56
	v_exp_f32 v57, v57
	v_exp_f32 v58, v58
	v_exp_f32 v59, v59
	v_exp_f32 v60, v60
	v_exp_f32 v61, v61
	v_exp_f32 v62, v62
	v_exp_f32 v63, v63
	v_smfmac_f32_32x32x32_bf16 v[16:31], v[88:91], v[48:55], v120
	s_setprio 0
	v_exp_f32 v32, v32
	s_setprio 1
	v_exp_f32 v33, v33
	v_exp_f32 v34, v34
	v_smfmac_f32_32x32x32_bf16 v[16:31], v[92:95], v[56:63], v120
	v_exp_f32 v35, v35
	v_exp_f32 v36, v36
	v_exp_f32 v37, v37
	v_mfma_f32_32x32x16_bf16 v[48:63], v[96:99], v[68:71], 0
	v_exp_f32 v38, v38
	v_exp_f32 v39, v39
	v_exp_f32 v40, v40
	v_exp_f32 v41, v41
	s_setprio 2
	v_exp_f32 v42, v42
	v_exp_f32 v43, v43
	v_exp_f32 v44, v44
	v_exp_f32 v45, v45
	v_exp_f32 v46, v46
	v_exp_f32 v47, v47
	s_waitcnt vmcnt(7)
	v_smfmac_f32_32x32x32_bf16 v[0:15], v[100:103], v[32:39], v120
	v_exp_f32 v48, v48
	v_exp_f32 v49, v49
	v_exp_f32 v50, v50
	v_smfmac_f32_32x32x32_bf16 v[0:15], v[104:107], v[40:47], v120
	v_exp_f32 v51, v51
	v_exp_f32 v52, v52
	v_exp_f32 v53, v53
	v_mfma_f32_32x32x16_bf16 v[32:47], v[108:111], v[64:67], 0
	buffer_load_dwordx4 v[248:251], v121, s[12:15], s39 offen offset:2048 sc1
	buffer_load_dwordx4 v[252:255], v121, s[12:15], s39 offen offset:3072 sc1
	v_exp_f32 v54, v54
	v_exp_f32 v55, v55
	v_exp_f32 v56, v56
	v_exp_f32 v57, v57
	v_exp_f32 v58, v58
	v_exp_f32 v59, v59
	v_exp_f32 v60, v60
	v_exp_f32 v61, v61
	v_exp_f32 v62, v62
	v_exp_f32 v63, v63
	v_smfmac_f32_32x32x32_bf16 v[16:31], v[100:103], v[48:55], v120
	s_setprio 0
	v_exp_f32 v32, v32
	s_setprio 1
	v_exp_f32 v33, v33
	v_exp_f32 v34, v34
	v_smfmac_f32_32x32x32_bf16 v[16:31], v[104:107], v[56:63], v120
	v_exp_f32 v35, v35
	v_exp_f32 v36, v36
	v_exp_f32 v37, v37
	v_mfma_f32_32x32x16_bf16 v[48:63], v[108:111], v[68:71], 0
	v_exp_f32 v38, v38
	v_exp_f32 v39, v39
	v_exp_f32 v40, v40
	v_exp_f32 v41, v41
	s_setprio 2
	v_exp_f32 v42, v42
	v_exp_f32 v43, v43
	v_exp_f32 v44, v44
	v_exp_f32 v45, v45
	v_exp_f32 v46, v46
	v_exp_f32 v47, v47
	s_waitcnt vmcnt(5)
	v_smfmac_f32_32x32x32_bf16 v[0:15], v[112:115], v[32:39], v120
	v_exp_f32 v48, v48
	v_exp_f32 v49, v49
	v_exp_f32 v50, v50
	v_smfmac_f32_32x32x32_bf16 v[0:15], v[116:119], v[40:47], v120
	v_exp_f32 v51, v51
	v_exp_f32 v52, v52
	v_exp_f32 v53, v53
	v_exp_f32 v54, v54
	v_exp_f32 v55, v55
	v_exp_f32 v56, v56
	v_exp_f32 v57, v57
	v_exp_f32 v58, v58
	v_exp_f32 v59, v59
	v_exp_f32 v60, v60
	v_exp_f32 v61, v61
	v_exp_f32 v62, v62
	v_exp_f32 v63, v63
	v_smfmac_f32_32x32x32_bf16 v[16:31], v[112:115], v[48:55], v120
	s_nop 0
	v_smfmac_f32_32x32x32_bf16 v[16:31], v[116:119], v[56:63], v120
.Lk_attdone:
	s_setprio 0
	v_mov_b32 v40, v123
	s_mul_i32 s59, s3, 3072
	s_add_u32 s59, s59, 131072
	v_add_u32 v123, s59, v121
	s_xor_b32 s59, s3, 4
	s_mul_i32 s59, s59, 3072
	s_add_u32 s59, s59, 131072
	v_add_u32 v127, s59, v121
	s_lshl_b32 s63, s3, 12
	s_add_u32 s44, s63, s54
	s_add_u32 s44, s44, 4194304
	s_cmp_eq_u32 s3, s29
	s_cselect_b32 s50, 1879048192, s44
	s_lshl_b32 s62, s29, 15
	s_and_b32 s62, s62, 32768
	s_add_u32 s44, s50, s62
	s_xor_b32 s48, s44, 0x8000
	ds_write_b128 v123, v[16:19]
	ds_write_b128 v123, v[20:23] offset:1024
	ds_write_b32 v123, v24 offset:2048
	s_and_b32 s59, s32, 0xffff
	s_add_u32 s59, s59, 65536
	v_add_u32 v59, s59, v121
	s_waitcnt vmcnt(0)
	s_cmp_lt_u32 s3, 4
	s_cbranch_scc1 .Lk_lowt
	s_branch .Lk_p16
.Lk_p15:
	buffer_load_dword v125, v124, s[12:15], s51 offen sc1
	s_waitcnt vmcnt(0)
.Lk_p16:
	v_cmp_eq_u32 vcc, 0, v125
	s_cbranch_vccz .Lk_p17
	s_sub_u32 s61, s61, 1
	s_cbranch_scc1 .Lk_end
	s_sleep 1
	s_branch .Lk_p15
.Lk_p17:
.Lk_attbar:
	s_waitcnt lgkmcnt(0)
	s_barrier
	ds_read_b32 v56, v127 offset:2048
	ds_read_b128 v[48:51], v127
	ds_read_b128 v[52:55], v127 offset:1024
	s_waitcnt lgkmcnt(2)
	v_add_f32 v56, v56, v8
	v_rcp_f32 v56, v56
	s_waitcnt lgkmcnt(1)
	v_add_f32 v0, v0, v48
	v_add_f32 v1, v1, v49
	v_add_f32 v2, v2, v50
	v_add_f32 v3, v3, v51
	s_waitcnt lgkmcnt(0)
	v_add_f32 v4, v4, v52
	v_add_f32 v5, v5, v53
	v_add_f32 v6, v6, v54
	v_add_f32 v7, v7, v55
	v_mul_f32 v0, v0, v56
	v_mul_f32 v1, v1, v56
	v_mul_f32 v2, v2, v56
	v_mul_f32 v3, v3, v56
	v_mul_f32 v4, v4, v56
	v_mul_f32 v5, v5, v56
	v_mul_f32 v6, v6, v56
	v_mul_f32 v7, v7, v56
	v_cvt_pk_bf16_f32 v60, v0, v1
	v_cvt_pk_bf16_f32 v61, v2, v3
	v_cvt_pk_bf16_f32 v62, v4, v5
	v_cvt_pk_bf16_f32 v63, v6, v7
	v_min_u32 v60, -2, v60
	v_min_u32 v61, -2, v61
	v_min_u32 v62, -2, v62
	v_min_u32 v63, -2, v63
	buffer_store_dwordx4 v[60:63], v121, s[12:15], s32 offen sc1
	ds_write_b128 v59, v[60:63]
	s_memrealtime s[26:27]
	v_add_u32 v48, 1, v125
	v_subrev_u32 v49, s24, v125
	v_cmp_lt_u32 vcc, 1, v48
	v_mov_b32 v48, 960
	s_lshl_b32 s59, s3, 3
	v_cndmask_b32 v49, v48, v49, vcc
	s_lshl_b32 s25, s29, 3
	s_nop 0
	v_add_u32_dpp v49, v49, v49 quad_perm:[1,0,3,2] row_mask:0xf bank_mask:0xf
	s_nop 1
	v_add_u32_dpp v49, v49, v49 quad_perm:[2,3,0,1] row_mask:0xf bank_mask:0xf
	s_nop 1
	v_add_u32_dpp v49, v49, v49 row_half_mirror row_mask:0xf bank_mask:0xf
	s_nop 0
	v_readlane_b32 s59, v49, s59
	v_readlane_b32 s25, v49, s25
	s_sub_i32 s59, s59, s25
	s_ashr_i32 s59, s59, 3
	s_min_i32 s59, s59, 120
	s_add_i32 s59, s59, 30
	s_cmp_gt_i32 s59, 50
	s_cselect_b32 s25, 0, s3
	s_waitcnt lgkmcnt(0)
	s_add_u32 s24, s26, s59
	s_sub_u32 s59, s3, s25
	s_cmp_lt_u32 s59, 4
	s_cbranch_scc1 .Lk_tw
	buffer_load_dwordx4 v[64:67], v121, s[12:15], s41 offen sc1
	buffer_load_dwordx4 v[68:71], v121, s[12:15], s41 offen offset:1024 sc1
	buffer_load_dwordx4 v[72:75], v121, s[12:15], s41 offen offset:2048 sc1
	buffer_load_dwordx4 v[76:79], v121, s[12:15], s41 offen offset:3072 sc1
	s_add_u32 s41, s41, 4096
	buffer_load_dwordx4 v[80:83], v121, s[12:15], s41 offen sc1
	buffer_load_dwordx4 v[84:87], v121, s[12:15], s41 offen offset:1024 sc1
	buffer_load_dwordx4 v[88:91], v121, s[12:15], s41 offen offset:2048 sc1
	buffer_load_dwordx4 v[92:95], v121, s[12:15], s41 offen offset:3072 sc1
	s_add_u32 s41, s41, 4096
	buffer_load_dwordx4 v[96:99], v121, s[12:15], s41 offen sc1
	buffer_load_dwordx4 v[100:103], v121, s[12:15], s41 offen offset:1024 sc1
	buffer_load_dwordx4 v[104:107], v121, s[12:15], s41 offen offset:2048 sc1
	buffer_load_dwordx4 v[108:111], v121, s[12:15], s41 offen offset:3072 sc1
	s_add_u32 s41, s41, 4096
	buffer_load_dwordx4 v[112:115], v121, s[12:15], s41 offen sc1
	buffer_load_dwordx4 v[116:119], v121, s[12:15], s41 offen offset:1024 sc1
.Lk_tw:
	s_sub_u32 s59, s26, s24
	s_cmp_ge_i32 s59, 0
	s_cbranch_scc1 .Lk_tgo
	s_memrealtime s[26:27]
	s_waitcnt lgkmcnt(0)
	s_branch .Lk_tw
.Lk_tgo:
	buffer_load_dwordx4 v[16:19], v121, s[12:15], s44 offen sc1
	buffer_load_dwordx4 v[20:23], v121, s[12:15], s44 offen offset:1024 sc1
	buffer_load_dwordx4 v[24:27], v121, s[12:15], s44 offen offset:2048 sc1
	buffer_load_dwordx4 v[28:31], v121, s[12:15], s44 offen offset:3072 sc1
	buffer_load_dwordx4 v[32:35], v121, s[12:15], s48 offen sc1
	buffer_load_dwordx4 v[36:39], v121, s[12:15], s48 offen offset:1024 sc1
	buffer_load_dwordx4 v[40:43], v121, s[12:15], s48 offen offset:2048 sc1
	buffer_load_dwordx4 v[44:47], v121, s[12:15], s48 offen offset:3072 sc1
	v_lshrrev_b32 v48, 2, v126
	v_xor_b32 v48, v48, v126
	v_and_b32 v48, 4, v48
	v_cmp_eq_u32 vcc, 0, v48
	v_mov_b32 v48, 0x3f003f00
	s_nop 0
	v_cndmask_b32 v52, 0, v48, vcc
	v_cndmask_b32 v53, 0, v48, vcc
	v_cndmask_b32 v54, 0, v48, vcc
	v_cndmask_b32 v55, 0, v48, vcc
	s_nop 1
	v_mfma_f32_16x16x32_bf16 v[48:51], v[52:55], v[128:131], 0
	s_add_u32 s63, s63, 65536
	v_add_u32 v58, s63, v121
	v_add_u32 v59, s62, v58
	v_xor_b32 v63, 0x8000, v59
	v_mfma_f32_16x16x32_bf16 v[48:51], v[52:55], v[132:135], v[48:51]
	s_and_b32 s59, s3, 1
	s_bfe_u32 s62, s3, 0x10001
	v_and_b32 v0, 31, v126
	v_lshrrev_b32 v1, 1, v0
	v_mfma_f32_16x16x32_bf16 v[48:51], v[52:55], v[136:139], v[48:51]
	s_lshl_b32 s63, s59, 2
	v_xor_b32 v1, s63, v1
	v_and_b32 v1, 7, v1
	v_lshlrev_b32 v1, 4, v1
	v_mfma_f32_16x16x32_bf16 v[48:51], v[52:55], v[140:143], v[48:51]
	v_lshl_or_b32 v0, v0, 7, v1
	v_lshrrev_b32 v60, 5, v126
	v_lshl_or_b32 v0, v60, 3, v0
	s_lshl_b32 s63, s62, 12
	v_mfma_f32_16x16x32_bf16 v[48:51], v[52:55], v[144:147], v[48:51]
	s_add_u32 s63, s63, 155648
	v_add_u32 v0, s63, v0
	v_xor_b32 v4, 16, v0
	v_xor_b32 v8, 32, v0
	v_mfma_f32_16x16x32_bf16 v[48:51], v[52:55], v[148:151], v[48:51]
	v_xor_b32 v12, 48, v0
	ds_read_b64 v[2:3], v0
	ds_read_b64 v[6:7], v4
	ds_read_b64 v[10:11], v8
	v_mfma_f32_16x16x32_bf16 v[48:51], v[52:55], v[152:155], v[48:51]
	ds_read_b64 v[14:15], v12
	v_bfe_u32 v56, v126, 1, 1
	v_bfe_u32 v57, v126, 4, 1
	v_lshl_or_b32 v56, v57, 1, v56
	v_mfma_f32_16x16x32_bf16 v[48:51], v[52:55], v[156:159], v[48:51]
	v_bfe_u32 v57, v126, 3, 1
	v_lshl_or_b32 v56, v57, 2, v56
	v_lshlrev_b32 v56, 4, v56
	v_and_b32 v57, 1, v126
	v_mfma_f32_16x16x32_bf16 v[48:51], v[52:55], v[160:163], v[48:51]
	v_lshl_or_b32 v56, v57, 3, v56
	v_bfe_u32 v57, v126, 2, 2
	v_lshl_or_b32 v56, v57, 7, v56
	v_lshl_or_b32 v56, v60, 9, v56
	v_mfma_f32_16x16x32_bf16 v[48:51], v[52:55], v[164:167], v[48:51]
	s_lshl_b32 s63, s59, 6
	v_xor_b32 v56, s63, v56
	s_lshl_b32 s63, s62, 15
	s_add_u32 s53, s63, 65536
	v_mfma_f32_16x16x32_bf16 v[48:51], v[52:55], v[168:171], v[48:51]
	v_add_u32 v57, s53, v121
	s_waitcnt lgkmcnt(0)
	v_lshlrev_b32 v0, 16, v2
	v_and_b32 v1, 0xffff0000, v2
	v_mfma_f32_16x16x32_bf16 v[48:51], v[52:55], v[172:175], v[48:51]
	v_lshlrev_b32 v2, 16, v3
	v_and_b32 v3, 0xffff0000, v3
	v_lshlrev_b32 v4, 16, v6
	v_and_b32 v5, 0xffff0000, v6
	v_mfma_f32_16x16x32_bf16 v[48:51], v[52:55], v[176:179], v[48:51]
	v_lshlrev_b32 v6, 16, v7
	v_and_b32 v7, 0xffff0000, v7
	v_lshlrev_b32 v8, 16, v10
	v_and_b32 v9, 0xffff0000, v10
	v_mfma_f32_16x16x32_bf16 v[48:51], v[52:55], v[180:183], v[48:51]
	v_lshlrev_b32 v10, 16, v11
	v_and_b32 v11, 0xffff0000, v11
	v_lshlrev_b32 v12, 16, v14
	v_and_b32 v13, 0xffff0000, v14
	v_mfma_f32_16x16x32_bf16 v[48:51], v[52:55], v[184:187], v[48:51]
	v_lshlrev_b32 v14, 16, v15
	v_and_b32 v15, 0xffff0000, v15
	s_add_u32 s53, s63, s54
	s_lshl_b32 s63, s29, 12
	v_mfma_f32_16x16x32_bf16 v[48:51], v[52:55], v[188:191], v[48:51]
	s_add_u32 s53, s53, s63
	s_lshl_b32 s59, s59, 11
	s_add_u32 s53, s53, s59
	s_add_u32 s53, s53, 6291456
	v_mfma_f32_16x16x32_bf16 v[48:51], v[52:55], v[192:195], v[48:51]
	s_lshl_b32 s59, s3, 8
	s_add_u32 s30, s30, s59
	s_mul_i32 s43, s3, 11264
	v_add_u32 v120, 90112, v121
	v_mfma_f32_16x16x32_bf16 v[48:51], v[52:55], v[196:199], v[48:51]
	v_lshlrev_b32 v127, 2, v126
	v_add_u32 v127, 155648, v127
	s_add_u32 s50, s50, 2097152
	s_mul_i32 s59, s3, 8448
	v_mfma_f32_16x16x32_bf16 v[48:51], v[52:55], v[200:203], v[48:51]
	v_lshl_add_u32 v122, v60, 4, v121
	v_add_u32 v122, s59, v122
	v_and_b32 v61, 15, v126
	v_lshrrev_b32 v62, 4, v126
	v_mfma_f32_16x16x32_bf16 v[48:51], v[52:55], v[204:207], v[48:51]
	s_lshl_b32 s59, s3, 2
	v_add_u32 v62, s59, v62
	v_lshlrev_b32 v124, 10, v62
	v_lshl_or_b32 v124, v61, 3, v124
	v_mfma_f32_16x16x32_bf16 v[48:51], v[52:55], v[208:211], v[48:51]
	v_mul_u32_u24 v123, 528, v61
	v_lshl_add_u32 v123, v62, 4, v123
	s_add_u32 s63, s63, 90112
	v_lshl_add_u32 v125, v62, 4, s63
	v_mfma_f32_16x16x32_bf16 v[48:51], v[52:55], v[212:215], v[48:51]
	v_lshrrev_b32 v62, 2, v61
	v_lshl_add_u32 v125, v62, 10, v125
	v_and_b32 v62, 1, v61
	v_lshl_add_u32 v125, v62, 9, v125
	v_mfma_f32_16x16x32_bf16 v[48:51], v[52:55], v[216:219], v[48:51]
	v_and_b32 v62, 2, v61
	v_lshl_add_u32 v125, v62, 2, v125
	s_mul_i32 s59, s3, 14336
	s_add_u32 s31, s31, s59
	v_mfma_f32_16x16x32_bf16 v[48:51], v[52:55], v[220:223], v[48:51]
	s_lshl_b32 s59, s29, 11
	s_cmp_eq_u32 s29, 7
	s_cselect_b32 s59, 0, s59
	s_add_u32 s36, s31, s59
	s_add_u32 s59, s59, 2048
	v_mfma_f32_16x16x32_bf16 v[48:51], v[52:55], v[224:227], v[48:51]
	s_cmp_eq_u32 s59, 14336
	s_cselect_b32 s59, 0, s59
	s_add_u32 s37, s31, s59
	s_add_u32 s59, s59, 2048
	s_cmp_eq_u32 s59, 14336
	s_cselect_b32 s59, 0, s59
	v_mfma_f32_16x16x32_bf16 v[48:51], v[52:55], v[228:231], v[48:51]
	s_add_u32 s38, s31, s59
	s_add_u32 s59, s59, 2048
	s_cmp_eq_u32 s59, 14336
	s_cselect_b32 s59, 0, s59
	s_add_u32 s39, s31, s59
	v_mfma_f32_16x16x32_bf16 v[48:51], v[52:55], v[232:235], v[48:51]
	s_add_u32 s59, s59, 2048
	s_cmp_eq_u32 s59, 14336
	s_cselect_b32 s59, 0, s59
	s_add_u32 s55, s31, s59
	s_add_u32 s59, s59, 2048
	v_mfma_f32_16x16x32_bf16 v[48:51], v[52:55], v[236:239], v[48:51]
	s_cmp_eq_u32 s59, 14336
	s_cselect_b32 s59, 0, s59
	s_add_u32 s56, s31, s59
	s_add_u32 s59, s59, 2048
	s_cmp_eq_u32 s59, 14336
	s_cselect_b32 s59, 0, s59
	v_mfma_f32_16x16x32_bf16 v[48:51], v[52:55], v[240:243], v[48:51]
	s_add_u32 s57, s31, s59
	v_mfma_f32_16x16x32_bf16 v[48:51], v[52:55], v[244:247], v[48:51]
	v_mfma_f32_16x16x32_bf16 v[48:51], v[52:55], v[248:251], v[48:51]
	s_cmp_lt_u32 s25, 4
	s_cbranch_scc1 .Lk_nowe
	buffer_load_dwordx4 v[64:67], v121, s[12:15], s41 offen sc1
	buffer_load_dwordx4 v[68:71], v121, s[12:15], s41 offen offset:1024 sc1
	buffer_load_dwordx4 v[72:75], v121, s[12:15], s41 offen offset:2048 sc1
	buffer_load_dwordx4 v[76:79], v121, s[12:15], s41 offen offset:3072 sc1
	s_add_u32 s41, s41, 4096
	buffer_load_dwordx4 v[80:83], v121, s[12:15], s41 offen sc1
	buffer_load_dwordx4 v[84:87], v121, s[12:15], s41 offen offset:1024 sc1
	buffer_load_dwordx4 v[88:91], v121, s[12:15], s41 offen offset:2048 sc1
	buffer_load_dwordx4 v[92:95], v121, s[12:15], s41 offen offset:3072 sc1
	s_add_u32 s41, s41, 4096
	buffer_load_dwordx4 v[96:99], v121, s[12:15], s41 offen sc1
	buffer_load_dwordx4 v[100:103], v121, s[12:15], s41 offen offset:1024 sc1
	buffer_load_dwordx4 v[104:107], v121, s[12:15], s41 offen offset:2048 sc1
	buffer_load_dwordx4 v[108:111], v121, s[12:15], s41 offen offset:3072 sc1
	s_add_u32 s41, s41, 4096
	buffer_load_dwordx4 v[112:115], v121, s[12:15], s41 offen sc1
	buffer_load_dwordx4 v[116:119], v121, s[12:15], s41 offen offset:1024 sc1
.Lk_nowe:
	v_mfma_f32_16x16x32_bf16 v[48:51], v[52:55], v[252:255], v[48:51]
	s_nop 11
	v_cvt_pk_bf16_f32 v49, v48, 0
	v_lshlrev_b32 v50, 16, v49
	v_sub_f32 v50, v48, v50
	v_cvt_pk_bf16_f32 v48, v48, v50
	v_mov_b32 v50, 0
	v_mov_b32 v51, 0
	s_cmp_eq_u32 s3, s29
	s_cbranch_scc1 .Lk_oown
	s_waitcnt vmcnt(14)
	s_cmp_ge_u32 s25, 4
	s_cbranch_scc1 .Lk_ochk2
.Lk_ochk:
	s_waitcnt vmcnt(0)
.Lk_ochk2:
	v_max3_u32 v60, v16, v17, v18
	v_max3_u32 v60, v60, v19, v20
	v_max3_u32 v60, v60, v21, v22
	v_max3_u32 v60, v60, v23, v24
	v_max3_u32 v60, v60, v25, v26
	v_max3_u32 v60, v60, v27, v28
	v_max3_u32 v60, v60, v29, v30
	v_max3_u32 v60, v60, v31, v32
	v_max3_u32 v60, v60, v33, v34
	v_max3_u32 v60, v60, v35, v36
	v_max3_u32 v60, v60, v37, v38
	v_max3_u32 v60, v60, v39, v40
	v_max3_u32 v60, v60, v41, v42
	v_max3_u32 v60, v60, v43, v44
	v_max3_u32 v60, v60, v45, v46
	v_max_u32 v60, v60, v47
	v_cmp_eq_u32 vcc, -1, v60
	s_cbranch_vccnz .Lk_oagain
	ds_write_b128 v59, v[16:19]
	ds_write_b128 v59, v[20:23] offset:1024
	ds_write_b128 v59, v[24:27] offset:2048
	ds_write_b128 v59, v[28:31] offset:3072
	ds_write_b128 v63, v[32:35]
	ds_write_b128 v63, v[36:39] offset:1024
	ds_write_b128 v63, v[40:43] offset:2048
	ds_write_b128 v63, v[44:47] offset:3072
.Lk_oown:
	s_waitcnt lgkmcnt(0)
	s_barrier
	s_cmp_ge_u32 s3, 4
	s_cbranch_scc1 .Lk_hiC
	ds_read_b64_tr_b16 v[16:17], v56
	ds_read_b64_tr_b16 v[18:19], v56 offset:1024
	ds_read_b128 v[20:23], v57
	ds_read_b64_tr_b16 v[24:25], v56 offset:2048
	ds_read_b64_tr_b16 v[26:27], v56 offset:3072
	ds_read_b128 v[28:31], v57 offset:1024
	ds_read_b64_tr_b16 v[32:33], v56 offset:4096
	ds_read_b64_tr_b16 v[34:35], v56 offset:5120
	ds_read_b128 v[36:39], v57 offset:2048
	s_waitcnt lgkmcnt(6)
	v_mfma_f32_32x32x16_bf16 v[0:15], v[16:19], v[20:23], v[0:15]
	ds_read_b64_tr_b16 v[40:41], v56 offset:6144
	ds_read_b64_tr_b16 v[42:43], v56 offset:7168
	ds_read_b128 v[44:47], v57 offset:3072
	s_waitcnt lgkmcnt(6)
	v_mfma_f32_32x32x16_bf16 v[0:15], v[24:27], v[28:31], v[0:15]
	ds_read_b64_tr_b16 v[16:17], v56 offset:8192
	ds_read_b64_tr_b16 v[18:19], v56 offset:9216
	ds_read_b128 v[20:23], v57 offset:4096
	s_waitcnt lgkmcnt(6)
	v_mfma_f32_32x32x16_bf16 v[0:15], v[32:35], v[36:39], v[0:15]
	ds_read_b64_tr_b16 v[24:25], v56 offset:10240
	ds_read_b64_tr_b16 v[26:27], v56 offset:11264
	ds_read_b128 v[28:31], v57 offset:5120
	s_waitcnt lgkmcnt(6)
	v_mfma_f32_32x32x16_bf16 v[0:15], v[40:43], v[44:47], v[0:15]
	ds_read_b64_tr_b16 v[32:33], v56 offset:12288
	ds_read_b64_tr_b16 v[34:35], v56 offset:13312
	ds_read_b128 v[36:39], v57 offset:6144
	s_waitcnt lgkmcnt(6)
	v_mfma_f32_32x32x16_bf16 v[0:15], v[16:19], v[20:23], v[0:15]
	ds_read_b64_tr_b16 v[40:41], v56 offset:14336
	ds_read_b64_tr_b16 v[42:43], v56 offset:15360
	ds_read_b128 v[44:47], v57 offset:7168
	s_waitcnt lgkmcnt(6)
	v_mfma_f32_32x32x16_bf16 v[0:15], v[24:27], v[28:31], v[0:15]
	ds_read_b64_tr_b16 v[16:17], v56 offset:16384
	ds_read_b64_tr_b16 v[18:19], v56 offset:17408
	ds_read_b128 v[20:23], v57 offset:8192
	s_waitcnt lgkmcnt(6)
	v_mfma_f32_32x32x16_bf16 v[0:15], v[32:35], v[36:39], v[0:15]
	ds_read_b64_tr_b16 v[24:25], v56 offset:18432
	ds_read_b64_tr_b16 v[26:27], v56 offset:19456
	ds_read_b128 v[28:31], v57 offset:9216
	s_waitcnt lgkmcnt(6)
	v_mfma_f32_32x32x16_bf16 v[0:15], v[40:43], v[44:47], v[0:15]
	ds_read_b64_tr_b16 v[32:33], v56 offset:20480
	ds_read_b64_tr_b16 v[34:35], v56 offset:21504
	ds_read_b128 v[36:39], v57 offset:10240
	s_waitcnt lgkmcnt(6)
	v_mfma_f32_32x32x16_bf16 v[0:15], v[16:19], v[20:23], v[0:15]
	ds_read_b64_tr_b16 v[40:41], v56 offset:22528
	ds_read_b64_tr_b16 v[42:43], v56 offset:23552
	ds_read_b128 v[44:47], v57 offset:11264
	s_waitcnt lgkmcnt(6)
	v_mfma_f32_32x32x16_bf16 v[0:15], v[24:27], v[28:31], v[0:15]
	ds_read_b64_tr_b16 v[16:17], v56 offset:24576
	ds_read_b64_tr_b16 v[18:19], v56 offset:25600
	ds_read_b128 v[20:23], v57 offset:12288
	s_waitcnt lgkmcnt(6)
	v_mfma_f32_32x32x16_bf16 v[0:15], v[32:35], v[36:39], v[0:15]
	ds_read_b64_tr_b16 v[24:25], v56 offset:26624
	ds_read_b64_tr_b16 v[26:27], v56 offset:27648
	ds_read_b128 v[28:31], v57 offset:13312
	s_waitcnt lgkmcnt(6)
	v_mfma_f32_32x32x16_bf16 v[0:15], v[40:43], v[44:47], v[0:15]
	ds_read_b64_tr_b16 v[32:33], v56 offset:28672
	ds_read_b64_tr_b16 v[34:35], v56 offset:29696
	ds_read_b128 v[36:39], v57 offset:14336
	s_waitcnt lgkmcnt(6)
	v_mfma_f32_32x32x16_bf16 v[0:15], v[16:19], v[20:23], v[0:15]
	ds_read_b64_tr_b16 v[40:41], v56 offset:30720
	ds_read_b64_tr_b16 v[42:43], v56 offset:31744
	ds_read_b128 v[44:47], v57 offset:15360
	s_waitcnt lgkmcnt(6)
	v_mfma_f32_32x32x16_bf16 v[0:15], v[24:27], v[28:31], v[0:15]
	ds_read_b64_tr_b16 v[16:17], v56 offset:32768
	ds_read_b64_tr_b16 v[18:19], v56 offset:33792
	ds_read_b128 v[20:23], v57 offset:16384
	s_waitcnt lgkmcnt(6)
	v_mfma_f32_32x32x16_bf16 v[0:15], v[32:35], v[36:39], v[0:15]
	ds_read_b64_tr_b16 v[24:25], v56 offset:34816
	ds_read_b64_tr_b16 v[26:27], v56 offset:35840
	ds_read_b128 v[28:31], v57 offset:17408
	s_waitcnt lgkmcnt(6)
	v_mfma_f32_32x32x16_bf16 v[0:15], v[40:43], v[44:47], v[0:15]
	ds_read_b64_tr_b16 v[32:33], v56 offset:36864
	ds_read_b64_tr_b16 v[34:35], v56 offset:37888
	ds_read_b128 v[36:39], v57 offset:18432
	s_waitcnt lgkmcnt(6)
	v_mfma_f32_32x32x16_bf16 v[0:15], v[16:19], v[20:23], v[0:15]
	ds_read_b64_tr_b16 v[40:41], v56 offset:38912
	ds_read_b64_tr_b16 v[42:43], v56 offset:39936
	ds_read_b128 v[44:47], v57 offset:19456
	s_waitcnt lgkmcnt(6)
	v_mfma_f32_32x32x16_bf16 v[0:15], v[24:27], v[28:31], v[0:15]
	ds_read_b64_tr_b16 v[16:17], v56 offset:40960
	ds_read_b64_tr_b16 v[18:19], v56 offset:41984
	ds_read_b128 v[20:23], v57 offset:20480
	s_waitcnt lgkmcnt(6)
	v_mfma_f32_32x32x16_bf16 v[0:15], v[32:35], v[36:39], v[0:15]
	ds_read_b64_tr_b16 v[24:25], v56 offset:43008
	ds_read_b64_tr_b16 v[26:27], v56 offset:44032
	ds_read_b128 v[28:31], v57 offset:21504
	s_waitcnt lgkmcnt(6)
	v_mfma_f32_32x32x16_bf16 v[0:15], v[40:43], v[44:47], v[0:15]
	ds_read_b64_tr_b16 v[32:33], v56 offset:45056
	ds_read_b64_tr_b16 v[34:35], v56 offset:46080
	ds_read_b128 v[36:39], v57 offset:22528
	s_waitcnt lgkmcnt(6)
	v_mfma_f32_32x32x16_bf16 v[0:15], v[16:19], v[20:23], v[0:15]
	ds_read_b64_tr_b16 v[40:41], v56 offset:47104
	ds_read_b64_tr_b16 v[42:43], v56 offset:48128
	ds_read_b128 v[44:47], v57 offset:23552
	s_waitcnt lgkmcnt(6)
	v_mfma_f32_32x32x16_bf16 v[0:15], v[24:27], v[28:31], v[0:15]
	ds_read_b64_tr_b16 v[16:17], v56 offset:49152
	ds_read_b64_tr_b16 v[18:19], v56 offset:50176
	ds_read_b128 v[20:23], v57 offset:24576
	s_waitcnt lgkmcnt(6)
	v_mfma_f32_32x32x16_bf16 v[0:15], v[32:35], v[36:39], v[0:15]
	ds_read_b64_tr_b16 v[24:25], v56 offset:51200
	ds_read_b64_tr_b16 v[26:27], v56 offset:52224
	ds_read_b128 v[28:31], v57 offset:25600
	s_waitcnt lgkmcnt(6)
	v_mfma_f32_32x32x16_bf16 v[0:15], v[40:43], v[44:47], v[0:15]
	ds_read_b64_tr_b16 v[32:33], v56 offset:53248
	ds_read_b64_tr_b16 v[34:35], v56 offset:54272
	ds_read_b128 v[36:39], v57 offset:26624
	s_waitcnt lgkmcnt(6)
	v_mfma_f32_32x32x16_bf16 v[0:15], v[16:19], v[20:23], v[0:15]
	ds_read_b64_tr_b16 v[40:41], v56 offset:55296
	ds_read_b64_tr_b16 v[42:43], v56 offset:56320
	ds_read_b128 v[44:47], v57 offset:27648
	s_waitcnt lgkmcnt(6)
	v_mfma_f32_32x32x16_bf16 v[0:15], v[24:27], v[28:31], v[0:15]
	ds_read_b64_tr_b16 v[16:17], v56 offset:57344
	ds_read_b64_tr_b16 v[18:19], v56 offset:58368
	ds_read_b128 v[20:23], v57 offset:28672
	s_waitcnt lgkmcnt(6)
	v_mfma_f32_32x32x16_bf16 v[0:15], v[32:35], v[36:39], v[0:15]
	ds_read_b64_tr_b16 v[24:25], v56 offset:59392
	ds_read_b64_tr_b16 v[26:27], v56 offset:60416
	ds_read_b128 v[28:31], v57 offset:29696
	s_waitcnt lgkmcnt(6)
	v_mfma_f32_32x32x16_bf16 v[0:15], v[40:43], v[44:47], v[0:15]
	ds_read_b64_tr_b16 v[32:33], v56 offset:61440
	ds_read_b64_tr_b16 v[34:35], v56 offset:62464
	ds_read_b128 v[36:39], v57 offset:30720
	s_waitcnt lgkmcnt(6)
	v_mfma_f32_32x32x16_bf16 v[0:15], v[16:19], v[20:23], v[0:15]
	ds_read_b64_tr_b16 v[40:41], v56 offset:63488
	ds_read_b64_tr_b16 v[42:43], v56 offset:64512
	ds_read_b128 v[44:47], v57 offset:31744
	s_waitcnt lgkmcnt(6)
	v_mfma_f32_32x32x16_bf16 v[0:15], v[24:27], v[28:31], v[0:15]
	s_waitcnt lgkmcnt(3)
	v_mfma_f32_32x32x16_bf16 v[0:15], v[32:35], v[36:39], v[0:15]
	s_waitcnt lgkmcnt(0)
	v_mfma_f32_32x32x16_bf16 v[0:15], v[40:43], v[44:47], v[0:15]
	s_and_b32 s59, s53, 0xffff
	v_add_u32 v56, s59, v120
	s_nop 9
	v_cvt_pk_bf16_f32 v32, v0, v1
	v_cvt_pk_bf16_f32 v33, v2, v3
	v_cvt_pk_bf16_f32 v34, v4, v5
	v_cvt_pk_bf16_f32 v35, v6, v7
	v_min_u32 v32, -2, v32
	v_min_u32 v33, -2, v33
	v_min_u32 v34, -2, v34
	v_min_u32 v35, -2, v35
	buffer_store_dwordx4 v[32:35], v121, s[12:15], s53 offen sc1
	v_cvt_pk_bf16_f32 v36, v8, v9
	v_cvt_pk_bf16_f32 v37, v10, v11
	v_cvt_pk_bf16_f32 v38, v12, v13
	v_cvt_pk_bf16_f32 v39, v14, v15
	v_min_u32 v36, -2, v36
	v_min_u32 v37, -2, v37
	v_min_u32 v38, -2, v38
	v_min_u32 v39, -2, v39
	buffer_store_dwordx4 v[36:39], v121, s[12:15], s53 offen offset:1024 sc1
	s_barrier
	v_pk_add_f32 v[16:17], v[0:1], v[2:3]
	v_pk_mul_f32 v[18:19], v[0:1], v[0:1]
	v_pk_fma_f32 v[18:19], v[2:3], v[2:3], v[18:19]
	v_pk_add_f32 v[16:17], v[16:17], v[4:5]
	v_pk_fma_f32 v[18:19], v[4:5], v[4:5], v[18:19]
	v_pk_add_f32 v[16:17], v[16:17], v[6:7]
	v_pk_fma_f32 v[18:19], v[6:7], v[6:7], v[18:19]
	v_pk_add_f32 v[16:17], v[16:17], v[8:9]
	v_pk_fma_f32 v[18:19], v[8:9], v[8:9], v[18:19]
	v_pk_add_f32 v[16:17], v[16:17], v[10:11]
	v_pk_fma_f32 v[18:19], v[10:11], v[10:11], v[18:19]
	v_pk_add_f32 v[16:17], v[16:17], v[12:13]
	v_pk_fma_f32 v[18:19], v[12:13], v[12:13], v[18:19]
	v_pk_add_f32 v[16:17], v[16:17], v[14:15]
	v_pk_fma_f32 v[18:19], v[14:15], v[14:15], v[18:19]
	v_add_f32 v16, v16, v17
	v_add_f32 v18, v18, v19
	v_lshlrev_b32 v17, 2, v126
	s_nop 0
	v_permlane32_swap_b32 v16, v18
	v_add_f32 v16, v16, v18
	v_min_u32 v16, -2, v16
	buffer_store_dword v16, v17, s[12:15], s30 offen sc1
	ds_write_b128 v56, v[32:35]
	ds_write_b128 v56, v[36:39] offset:1024
	s_add_u32 s41, s41, 2048
	buffer_load_dwordx4 v[16:19], v121, s[12:15], s41 offen sc1
	buffer_load_dwordx4 v[20:23], v121, s[12:15], s41 offen offset:1024 sc1
	buffer_load_dwordx4 v[24:27], v121, s[12:15], s41 offen offset:2048 sc1
	buffer_load_dwordx4 v[28:31], v121, s[12:15], s41 offen offset:3072 sc1
	s_add_u32 s41, s41, 4096
	buffer_load_dwordx4 v[40:43], v121, s[12:15], s41 offen sc1
	buffer_load_dwordx4 v[44:47], v121, s[12:15], s41 offen offset:1024 sc1
	buffer_load_dwordx4 v[52:55], v121, s[12:15], s41 offen offset:2048 sc1
	buffer_load_dwordx4 v[60:63], v121, s[12:15], s41 offen offset:3072 sc1
	s_add_u32 s41, s41, 4096
	v_add_u32 v59, s43, v121
	s_sleep 8
	buffer_load_dwordx4 v[0:3], v121, s[12:15], s50 offen sc1
	buffer_load_dwordx4 v[4:7], v121, s[12:15], s50 offen offset:1024 sc1
	buffer_load_dwordx4 v[8:11], v121, s[12:15], s50 offen offset:2048 sc1
	buffer_load_dwordx4 v[12:15], v121, s[12:15], s50 offen offset:3072 sc1
	s_waitcnt vmcnt(11)
	ds_write_b128 v59, v[16:19]
	s_waitcnt vmcnt(10)
	ds_write_b128 v59, v[20:23] offset:1024
	s_waitcnt vmcnt(9)
	ds_write_b128 v59, v[24:27] offset:2048
	s_waitcnt vmcnt(8)
	ds_write_b128 v59, v[28:31] offset:3072
	s_waitcnt vmcnt(7)
	ds_write_b128 v59, v[40:43] offset:4096
	s_waitcnt vmcnt(6)
	ds_write_b128 v59, v[44:47] offset:5120
	s_waitcnt vmcnt(5)
	ds_write_b128 v59, v[52:55] offset:6144
	s_waitcnt vmcnt(4)
	ds_write_b128 v59, v[60:63] offset:7168
.Lk_hld0:
	s_waitcnt vmcnt(0)
	s_cmp_eq_u32 s3, s29
	s_cbranch_scc1 .Lk_hown0
	v_max3_u32 v57, v0, v1, v2
	v_max3_u32 v57, v57, v3, v4
	v_max3_u32 v57, v57, v5, v6
	v_max3_u32 v57, v57, v7, v8
	v_max3_u32 v57, v57, v9, v10
	v_max3_u32 v57, v57, v11, v12
	v_max3_u32 v57, v57, v13, v14
	v_max_u32 v57, v57, v15
	v_cmp_eq_u32 vcc, -1, v57
	s_cbranch_vccnz .Lk_hagain0
	ds_write_b128 v58, v[0:3] offset:24576
	ds_write_b128 v58, v[4:7] offset:25600
	ds_write_b128 v58, v[8:11] offset:26624
	ds_write_b128 v58, v[12:15] offset:27648
.Lk_hown0:
	s_waitcnt lgkmcnt(0)
	s_barrier
.Lk_D:
	s_setprio 3
	ds_read_b128 v[32:35], v120
	ds_read_b128 v[36:39], v120 offset:1024
	ds_read_b128 v[40:43], v120 offset:2048
	s_waitcnt lgkmcnt(2)
	v_mfma_f32_32x32x16_bf16 v[0:15], v[128:131], v[32:35], 0
	ds_read_b128 v[44:47], v120 offset:3072
	s_waitcnt lgkmcnt(2)
	v_mfma_f32_32x32x16_bf16 v[0:15], v[132:135], v[36:39], v[0:15]
	ds_read_b128 v[32:35], v120 offset:4096
	s_waitcnt lgkmcnt(2)
	v_mfma_f32_32x32x16_bf16 v[0:15], v[136:139], v[40:43], v[0:15]
	ds_read_b128 v[36:39], v120 offset:5120
	s_waitcnt lgkmcnt(2)
	v_mfma_f32_32x32x16_bf16 v[0:15], v[140:143], v[44:47], v[0:15]
	ds_read_b128 v[40:43], v120 offset:6144
	s_waitcnt lgkmcnt(2)
	v_mfma_f32_32x32x16_bf16 v[0:15], v[144:147], v[32:35], v[0:15]
	ds_read_b128 v[44:47], v120 offset:7168
	s_waitcnt lgkmcnt(2)
	v_mfma_f32_32x32x16_bf16 v[0:15], v[148:151], v[36:39], v[0:15]
	ds_read_b128 v[32:35], v120 offset:8192
	s_waitcnt lgkmcnt(2)
	v_mfma_f32_32x32x16_bf16 v[0:15], v[152:155], v[40:43], v[0:15]
	ds_read_b128 v[36:39], v120 offset:9216
	s_waitcnt lgkmcnt(2)
	v_mfma_f32_32x32x16_bf16 v[0:15], v[156:159], v[44:47], v[0:15]
	ds_read_b128 v[40:43], v120 offset:10240
	s_waitcnt lgkmcnt(2)
	v_mfma_f32_32x32x16_bf16 v[0:15], v[160:163], v[32:35], v[0:15]
	ds_read_b128 v[44:47], v120 offset:11264
	s_waitcnt lgkmcnt(2)
	v_mfma_f32_32x32x16_bf16 v[0:15], v[164:167], v[36:39], v[0:15]
	ds_read_b128 v[32:35], v120 offset:12288
	s_waitcnt lgkmcnt(2)
	v_mfma_f32_32x32x16_bf16 v[0:15], v[168:171], v[40:43], v[0:15]
	ds_read_b128 v[36:39], v120 offset:13312
	s_waitcnt lgkmcnt(2)
	v_mfma_f32_32x32x16_bf16 v[0:15], v[172:175], v[44:47], v[0:15]
	ds_read_b128 v[40:43], v120 offset:14336
	s_waitcnt lgkmcnt(2)
	v_mfma_f32_32x32x16_bf16 v[0:15], v[176:179], v[32:35], v[0:15]
	ds_read_b128 v[44:47], v120 offset:15360
	s_waitcnt lgkmcnt(2)
	v_mfma_f32_32x32x16_bf16 v[0:15], v[180:183], v[36:39], v[0:15]
	ds_read_b128 v[32:35], v120 offset:16384
	s_waitcnt lgkmcnt(2)
	v_mfma_f32_32x32x16_bf16 v[0:15], v[184:187], v[40:43], v[0:15]
	ds_read_b128 v[36:39], v120 offset:17408
	s_waitcnt lgkmcnt(2)
	v_mfma_f32_32x32x16_bf16 v[0:15], v[188:191], v[44:47], v[0:15]
	ds_read_b128 v[40:43], v120 offset:18432
	s_waitcnt lgkmcnt(2)
	v_mfma_f32_32x32x16_bf16 v[0:15], v[192:195], v[32:35], v[0:15]
	ds_read_b128 v[44:47], v120 offset:19456
	s_waitcnt lgkmcnt(2)
	v_mfma_f32_32x32x16_bf16 v[0:15], v[196:199], v[36:39], v[0:15]
	ds_read_b128 v[32:35], v120 offset:20480
	s_waitcnt lgkmcnt(2)
	v_mfma_f32_32x32x16_bf16 v[0:15], v[200:203], v[40:43], v[0:15]
	ds_read_b128 v[36:39], v120 offset:21504
	s_waitcnt lgkmcnt(2)
	v_mfma_f32_32x32x16_bf16 v[0:15], v[204:207], v[44:47], v[0:15]
	ds_read_b128 v[40:43], v120 offset:22528
	s_waitcnt lgkmcnt(2)
	v_mfma_f32_32x32x16_bf16 v[0:15], v[208:211], v[32:35], v[0:15]
	ds_read_b128 v[44:47], v120 offset:23552
	s_waitcnt lgkmcnt(2)
	v_mfma_f32_32x32x16_bf16 v[0:15], v[212:215], v[36:39], v[0:15]
	ds_read_b128 v[32:35], v120 offset:24576
	s_waitcnt lgkmcnt(2)
	v_mfma_f32_32x32x16_bf16 v[0:15], v[216:219], v[40:43], v[0:15]
	ds_read_b128 v[36:39], v120 offset:25600
	s_waitcnt lgkmcnt(2)
	v_mfma_f32_32x32x16_bf16 v[0:15], v[220:223], v[44:47], v[0:15]
	ds_read_b128 v[40:43], v120 offset:26624
	s_waitcnt lgkmcnt(2)
	v_mfma_f32_32x32x16_bf16 v[0:15], v[224:227], v[32:35], v[0:15]
	ds_read_b128 v[44:47], v120 offset:27648
	s_waitcnt lgkmcnt(2)
	v_mfma_f32_32x32x16_bf16 v[0:15], v[228:231], v[36:39], v[0:15]
	ds_read_b128 v[32:35], v120 offset:28672
	s_waitcnt lgkmcnt(2)
	v_mfma_f32_32x32x16_bf16 v[0:15], v[232:235], v[40:43], v[0:15]
	ds_read_b128 v[36:39], v120 offset:29696
	s_cmp_ge_u32 s3, 4
	s_cbranch_scc1 .Lk_cb18
	s_barrier
.Lk_cb18:
	s_waitcnt lgkmcnt(2)
	v_mfma_f32_32x32x16_bf16 v[0:15], v[236:239], v[44:47], v[0:15]
	ds_read_b128 v[40:43], v120 offset:30720
	ds_read2st64_b32 v[16:17], v127 offset0:0 offset1:1
	ds_read2st64_b32 v[18:19], v127 offset0:4 offset1:5
	ds_read2st64_b32 v[20:21], v127 offset0:8 offset1:9
	ds_read2st64_b32 v[22:23], v127 offset0:12 offset1:13
	s_waitcnt lgkmcnt(6)
	v_mfma_f32_32x32x16_bf16 v[0:15], v[240:243], v[32:35], v[0:15]
	ds_read_b128 v[44:47], v120 offset:31744
	ds_read2st64_b32 v[24:25], v127 offset0:16 offset1:17
	ds_read2st64_b32 v[26:27], v127 offset0:20 offset1:21
	ds_read2st64_b32 v[28:29], v127 offset0:24 offset1:25
	ds_read2st64_b32 v[30:31], v127 offset0:28 offset1:29
	s_waitcnt lgkmcnt(10)
	v_mfma_f32_32x32x16_bf16 v[0:15], v[244:247], v[36:39], v[0:15]
	ds_read_b128 v[32:35], v120 offset:32768
	v_mov_b32 v54, 0
	v_mov_b32 v55, 0
	v_add_u32 v62, s43, v121
	s_waitcnt lgkmcnt(1)
	v_mfma_f32_32x32x16_bf16 v[0:15], v[248:251], v[40:43], v[0:15]
	ds_read_b128 v[36:39], v120 offset:33792
	v_add_f32 v16, v16, v24
	v_add_f32 v17, v17, v25
	v_add_f32 v18, v18, v26
	v_add_f32 v19, v19, v27
	v_mfma_f32_32x32x16_bf16 v[0:15], v[252:255], v[44:47], v[0:15]
	ds_read_b128 v[40:43], v120 offset:34816
	v_add_f32 v20, v20, v28
	v_add_f32 v21, v21, v29
	v_add_f32 v22, v22, v30
	v_add_f32 v23, v23, v31
	v_add_f32 v16, v16, v20
	v_add_f32 v17, v17, v21
	v_add_f32 v18, v18, v22
	v_add_f32 v19, v19, v23
	v_add_f32 v16, v16, v18
	v_add_f32 v17, v17, v19
	v_add_f32 v63, v16, v17
	v_add_f32 v60, v16, v17
	s_nop 1
	v_permlane32_swap_b32 v63, v60
	v_mul_f32 v63, 0xbb000000, v63
	v_mul_f32 v16, v63, v63
	v_fma_f32 v60, v60, s49, -v16
	v_max_f32 v60, 0, v60
	v_add_f32 v60, 0x3727c5ac, v60
	v_rsq_f32 v60, v60
	v_cvt_pk_bf16_f32 v52, v63, v63
	v_lshlrev_b32 v53, 16, v52
	v_sub_f32 v53, v63, v53
	v_cvt_pk_bf16_f32 v53, v53, 0
	s_nop 1
	v_mfma_f32_32x32x16_bf16 v[0:15], v[48:51], v[52:55], v[0:15]
	s_nop 11
	v_cvt_pk_bf16_f32 v0, v0, v1
	v_cvt_pk_bf16_f32 v1, v2, v3
	v_cvt_pk_bf16_f32 v2, v4, v5
	v_cvt_pk_bf16_f32 v3, v6, v7
	v_pk_max_i16 v0, v0, 0
	v_pk_max_i16 v1, v1, 0
	v_pk_max_i16 v2, v2, 0
	v_pk_max_i16 v3, v3, 0
	buffer_store_dwordx4 v[0:3], v121, s[12:15], s35 offen sc1
	v_cvt_pk_bf16_f32 v4, v8, v9
	v_cvt_pk_bf16_f32 v5, v10, v11
	v_cvt_pk_bf16_f32 v6, v12, v13
	v_cvt_pk_bf16_f32 v7, v14, v15
	v_pk_max_i16 v4, v4, 0
	v_pk_max_i16 v5, v5, 0
	v_pk_max_i16 v6, v6, 0
	v_pk_max_i16 v7, v7, 0
	buffer_store_dwordx4 v[4:7], v121, s[12:15], s35 offen offset:1024 sc1
	s_setprio 2
	ds_read2st64_b32 v[16:17], v127 offset0:2 offset1:3
	ds_read2st64_b32 v[18:19], v127 offset0:6 offset1:7
	ds_read2st64_b32 v[20:21], v127 offset0:10 offset1:11
	ds_read2st64_b32 v[22:23], v127 offset0:14 offset1:15
	ds_read2st64_b32 v[24:25], v127 offset0:18 offset1:19
	ds_read2st64_b32 v[26:27], v127 offset0:22 offset1:23
	ds_read2st64_b32 v[28:29], v127 offset0:26 offset1:27
	ds_read2st64_b32 v[30:31], v127 offset0:30 offset1:31
	s_waitcnt lgkmcnt(0)
	v_add_f32 v16, v16, v24
	v_add_f32 v17, v17, v25
	v_add_f32 v18, v18, v26
	v_add_f32 v19, v19, v27
	v_add_f32 v20, v20, v28
	v_add_f32 v21, v21, v29
	v_add_f32 v22, v22, v30
	v_add_f32 v23, v23, v31
	v_add_f32 v16, v16, v20
	v_add_f32 v17, v17, v21
	v_add_f32 v18, v18, v22
	v_add_f32 v19, v19, v23
	v_add_f32 v16, v16, v18
	v_add_f32 v17, v17, v19
	v_add_f32 v63, v16, v17
	v_add_f32 v61, v16, v17
	s_nop 1
	v_permlane32_swap_b32 v63, v61
	v_mul_f32 v63, 0xbb000000, v63
	v_mul_f32 v16, v63, v63
	v_fma_f32 v61, v61, s49, -v16
	v_max_f32 v61, 0, v61
	v_add_f32 v61, 0x3727c5ac, v61
	v_rsq_f32 v61, v61
	v_cvt_pk_bf16_f32 v52, v63, v63
	v_lshlrev_b32 v53, 16, v52
	v_sub_f32 v53, v63, v53
	v_cvt_pk_bf16_f32 v53, v53, 0
	v_mfma_f32_32x32x16_bf16 v[16:31], v[128:131], v[32:35], 0
	ds_read_b128 v[44:47], v120 offset:35840
	v_add_u32 v56, 114688, v121
	s_add_u32 s43, s42, 16384
	s_add_u32 s44, s42, 32768
	v_mfma_f32_32x32x16_bf16 v[16:31], v[132:135], v[36:39], v[16:31]
	ds_read_b128 v[32:35], v120 offset:36864
	v_lshrrev_b32 v57, 10, v124
	v_lshlrev_b32 v57, 2, v57
	v_mfma_f32_32x32x16_bf16 v[16:31], v[136:139], v[40:43], v[16:31]
	ds_read_b128 v[36:39], v120 offset:37888
	ds_read_b128 v[128:131], v62
	s_waitcnt lgkmcnt(3)
	v_mfma_f32_32x32x16_bf16 v[16:31], v[140:143], v[44:47], v[16:31]
	ds_read_b128 v[40:43], v120 offset:38912
	ds_read_b128 v[132:135], v62 offset:1024
	s_waitcnt lgkmcnt(4)
	v_mfma_f32_32x32x16_bf16 v[16:31], v[144:147], v[32:35], v[16:31]
	ds_read_b128 v[44:47], v120 offset:39936
	ds_read_b128 v[136:139], v62 offset:2048
	s_waitcnt lgkmcnt(5)
	v_mfma_f32_32x32x16_bf16 v[16:31], v[148:151], v[36:39], v[16:31]
	ds_read_b128 v[32:35], v120 offset:40960
	ds_read_b128 v[140:143], v62 offset:3072
	s_waitcnt lgkmcnt(5)
	v_mfma_f32_32x32x16_bf16 v[16:31], v[152:155], v[40:43], v[16:31]
	ds_read_b128 v[36:39], v120 offset:41984
	ds_read_b128 v[144:147], v62 offset:4096
	s_waitcnt lgkmcnt(5)
	v_mfma_f32_32x32x16_bf16 v[16:31], v[156:159], v[44:47], v[16:31]
	ds_read_b128 v[40:43], v120 offset:43008
	ds_read_b128 v[148:151], v62 offset:5120
	s_waitcnt lgkmcnt(5)
	v_mfma_f32_32x32x16_bf16 v[16:31], v[160:163], v[32:35], v[16:31]
	ds_read_b128 v[44:47], v120 offset:44032
	ds_read_b128 v[152:155], v62 offset:6144
	s_waitcnt lgkmcnt(5)
	v_mfma_f32_32x32x16_bf16 v[16:31], v[164:167], v[36:39], v[16:31]
	ds_read_b128 v[32:35], v120 offset:45056
	ds_read_b128 v[156:159], v62 offset:7168
	s_waitcnt lgkmcnt(5)
	v_mfma_f32_32x32x16_bf16 v[16:31], v[168:171], v[40:43], v[16:31]
	ds_read_b128 v[36:39], v120 offset:46080
	ds_read_b128 v[160:163], v62 offset:8192
	s_waitcnt lgkmcnt(5)
	v_mfma_f32_32x32x16_bf16 v[16:31], v[172:175], v[44:47], v[16:31]
	ds_read_b128 v[40:43], v120 offset:47104
	ds_read_b128 v[164:167], v62 offset:9216
	s_waitcnt lgkmcnt(5)
	v_mfma_f32_32x32x16_bf16 v[16:31], v[176:179], v[32:35], v[16:31]
	ds_read_b128 v[44:47], v120 offset:48128
	ds_read_b128 v[168:171], v62 offset:10240
	buffer_load_dwordx4 v[172:175], v121, s[12:15], s41 offen offset:3072 sc1
	s_waitcnt lgkmcnt(5)
	v_mfma_f32_32x32x16_bf16 v[16:31], v[180:183], v[36:39], v[16:31]
	ds_read_b128 v[32:35], v120 offset:49152
	s_waitcnt lgkmcnt(4)
	v_mfma_f32_32x32x16_bf16 v[16:31], v[184:187], v[40:43], v[16:31]
	ds_read_b128 v[36:39], v120 offset:50176
	s_add_u32 s41, s41, 4096
	buffer_load_dwordx4 v[176:179], v121, s[12:15], s41 offen sc1
	s_waitcnt lgkmcnt(3)
	v_mfma_f32_32x32x16_bf16 v[16:31], v[188:191], v[44:47], v[16:31]
	ds_read_b128 v[40:43], v120 offset:51200
	s_waitcnt lgkmcnt(2)
	v_mfma_f32_32x32x16_bf16 v[16:31], v[192:195], v[32:35], v[16:31]
	ds_read_b128 v[44:47], v120 offset:52224
	buffer_load_dwordx4 v[180:183], v121, s[12:15], s41 offen offset:1024 sc1
	s_waitcnt lgkmcnt(2)
	v_mfma_f32_32x32x16_bf16 v[16:31], v[196:199], v[36:39], v[16:31]
	ds_read_b128 v[32:35], v120 offset:53248
	s_waitcnt lgkmcnt(2)
	v_mfma_f32_32x32x16_bf16 v[16:31], v[200:203], v[40:43], v[16:31]
	ds_read_b128 v[36:39], v120 offset:54272
	s_waitcnt lgkmcnt(2)
	v_mfma_f32_32x32x16_bf16 v[16:31], v[204:207], v[44:47], v[16:31]
	ds_read_b128 v[40:43], v120 offset:55296
	s_cmp_lg_u32 s29, 7
	s_cbranch_scc1 .Lk_f19
	buffer_load_dwordx4 v[0:3], v121, s[12:15], s36 offen sc1
	buffer_load_dwordx4 v[4:7], v121, s[12:15], s36 offen offset:1024 sc1
.Lk_f19:
	buffer_load_dwordx4 v[184:187], v121, s[12:15], s37 offen sc1
	s_waitcnt lgkmcnt(2)
	v_mfma_f32_32x32x16_bf16 v[16:31], v[208:211], v[32:35], v[16:31]
	ds_read_b128 v[44:47], v120 offset:56320
	buffer_load_dwordx4 v[188:191], v121, s[12:15], s37 offen offset:1024 sc1
	s_waitcnt lgkmcnt(2)
	v_mfma_f32_32x32x16_bf16 v[16:31], v[212:215], v[36:39], v[16:31]
	ds_read_b128 v[32:35], v120 offset:57344
	buffer_load_dwordx4 v[192:195], v121, s[12:15], s38 offen sc1
	s_waitcnt lgkmcnt(2)
	v_mfma_f32_32x32x16_bf16 v[16:31], v[216:219], v[40:43], v[16:31]
	ds_read_b128 v[36:39], v120 offset:58368
	buffer_load_dwordx4 v[196:199], v121, s[12:15], s38 offen offset:1024 sc1
	s_waitcnt lgkmcnt(2)
	v_mfma_f32_32x32x16_bf16 v[16:31], v[220:223], v[44:47], v[16:31]
	ds_read_b128 v[40:43], v120 offset:59392
	buffer_load_dwordx4 v[200:203], v121, s[12:15], s39 offen sc1
	s_waitcnt lgkmcnt(2)
	v_mfma_f32_32x32x16_bf16 v[16:31], v[224:227], v[32:35], v[16:31]
	ds_read_b128 v[44:47], v120 offset:60416
	buffer_load_dwordx4 v[204:207], v121, s[12:15], s39 offen offset:1024 sc1
	s_waitcnt lgkmcnt(2)
	v_mfma_f32_32x32x16_bf16 v[16:31], v[228:231], v[36:39], v[16:31]
	ds_read_b128 v[32:35], v120 offset:61440
	buffer_load_dwordx4 v[208:211], v121, s[12:15], s55 offen sc1
	s_waitcnt lgkmcnt(2)
	v_mfma_f32_32x32x16_bf16 v[16:31], v[232:235], v[40:43], v[16:31]
	ds_read_b128 v[36:39], v120 offset:62464
	buffer_load_dwordx4 v[212:215], v121, s[12:15], s55 offen offset:1024 sc1
	s_waitcnt lgkmcnt(2)
	v_mfma_f32_32x32x16_bf16 v[16:31], v[236:239], v[44:47], v[16:31]
	ds_read_b128 v[40:43], v120 offset:63488
	buffer_load_dwordx4 v[216:219], v121, s[12:15], s56 offen sc1
	s_waitcnt lgkmcnt(2)
	v_mfma_f32_32x32x16_bf16 v[16:31], v[240:243], v[32:35], v[16:31]
	ds_read_b128 v[44:47], v120 offset:64512
	buffer_load_dwordx4 v[220:223], v121, s[12:15], s56 offen offset:1024 sc1
	s_waitcnt lgkmcnt(2)
	v_mfma_f32_32x32x16_bf16 v[16:31], v[244:247], v[36:39], v[16:31]
	buffer_load_dwordx4 v[224:227], v121, s[12:15], s57 offen sc1
	s_waitcnt lgkmcnt(1)
	v_mfma_f32_32x32x16_bf16 v[16:31], v[248:251], v[40:43], v[16:31]
	buffer_load_dwordx4 v[228:231], v121, s[12:15], s57 offen offset:1024 sc1
	s_waitcnt lgkmcnt(0)
	v_mfma_f32_32x32x16_bf16 v[16:31], v[252:255], v[44:47], v[16:31]
	v_mfma_f32_32x32x16_bf16 v[16:31], v[48:51], v[52:55], v[16:31]
	ds_read_b64 v[48:49], v125
	ds_read_b64 v[50:51], v125 offset:256
	ds_read_b64 v[52:53], v125 offset:32768
	ds_bpermute_b32 v54, v57, v60
	ds_bpermute_b32 v55, v57, v60 offset:64
	ds_bpermute_b32 v57, v57, v61
	s_nop 5
	v_cvt_pk_bf16_f32 v16, v16, v17
	v_cvt_pk_bf16_f32 v17, v18, v19
	v_cvt_pk_bf16_f32 v18, v20, v21
	v_cvt_pk_bf16_f32 v19, v22, v23
	v_pk_max_i16 v16, v16, 0
	v_pk_max_i16 v17, v17, 0
	v_pk_max_i16 v18, v18, 0
	v_pk_max_i16 v19, v19, 0
	buffer_store_dwordx4 v[16:19], v121, s[12:15], s45 offen sc1
	v_cvt_pk_bf16_f32 v20, v24, v25
	v_cvt_pk_bf16_f32 v21, v26, v27
	v_cvt_pk_bf16_f32 v22, v28, v29
	v_cvt_pk_bf16_f32 v23, v30, v31
	v_pk_max_i16 v20, v20, 0
	v_pk_max_i16 v21, v21, 0
	v_pk_max_i16 v22, v22, 0
	v_pk_max_i16 v23, v23, 0
	buffer_store_dwordx4 v[20:23], v121, s[12:15], s45 offen offset:1024 sc1
	s_setprio 1
	s_waitcnt lgkmcnt(0)
	s_cmp_ge_u32 s3, 4
	s_cbranch_scc0 .Lk_cb20
	s_barrier
.Lk_cb20:
	s_cmp_lg_u32 s29, 7
	s_cbranch_scc1 .Lk_f21
	s_waitcnt vmcnt(14)
.Lk_c00:
	v_max3_u32 v63, v0, v1, v2
	v_max3_u32 v63, v63, v3, v4
	v_max3_u32 v63, v63, v5, v6
	v_max_u32 v63, v63, v7
	v_cmp_eq_u32 vcc, -1, v63
	s_cbranch_vccnz .Lk_s00
.Lk_f21:
	v_mfma_f32_32x32x16_bf16 v[240:255], v[64:67], v[0:3], 0
	v_mfma_f32_32x32x16_bf16 v[32:47], v[72:75], v[0:3], 0
	s_waitcnt vmcnt(12)
	v_max3_u32 v63, v184, v185, v186
	v_max3_u32 v63, v63, v187, v188
	v_mfma_f32_32x32x16_bf16 v[240:255], v[68:71], v[4:7], v[240:255]
	v_max3_u32 v63, v63, v189, v190
	v_max_u32 v63, v63, v191
	v_cmp_eq_u32 vcc, -1, v63
	v_mfma_f32_32x32x16_bf16 v[32:47], v[76:79], v[4:7], v[32:47]
	s_cbranch_vccnz .Lk_s01
.Lk_c01:
	v_mfma_f32_32x32x16_bf16 v[240:255], v[80:83], v[184:187], v[240:255]
	v_mfma_f32_32x32x16_bf16 v[32:47], v[88:91], v[184:187], v[32:47]
	s_waitcnt vmcnt(10)
	v_max3_u32 v63, v192, v193, v194
	v_max3_u32 v63, v63, v195, v196
	v_mfma_f32_32x32x16_bf16 v[240:255], v[84:87], v[188:191], v[240:255]
	v_max3_u32 v63, v63, v197, v198
	v_max_u32 v63, v63, v199
	v_cmp_eq_u32 vcc, -1, v63
	v_mfma_f32_32x32x16_bf16 v[32:47], v[92:95], v[188:191], v[32:47]
	s_cbranch_vccnz .Lk_s02
.Lk_c02:
	v_mfma_f32_32x32x16_bf16 v[240:255], v[96:99], v[192:195], v[240:255]
	v_mfma_f32_32x32x16_bf16 v[32:47], v[104:107], v[192:195], v[32:47]
	s_waitcnt vmcnt(8)
	v_max3_u32 v63, v200, v201, v202
	v_max3_u32 v63, v63, v203, v204
	v_mfma_f32_32x32x16_bf16 v[240:255], v[100:103], v[196:199], v[240:255]
	v_max3_u32 v63, v63, v205, v206
	v_max_u32 v63, v63, v207
	v_cmp_eq_u32 vcc, -1, v63
	v_mfma_f32_32x32x16_bf16 v[32:47], v[108:111], v[196:199], v[32:47]
	s_cbranch_vccnz .Lk_s03
.Lk_c03:
	v_mfma_f32_32x32x16_bf16 v[240:255], v[112:115], v[200:203], v[240:255]
	v_mfma_f32_32x32x16_bf16 v[32:47], v[128:131], v[200:203], v[32:47]
	s_waitcnt vmcnt(6)
	v_max3_u32 v63, v208, v209, v210
	v_max3_u32 v63, v63, v211, v212
	v_mfma_f32_32x32x16_bf16 v[240:255], v[116:119], v[204:207], v[240:255]
	v_max3_u32 v63, v63, v213, v214
	v_max_u32 v63, v63, v215
	v_cmp_eq_u32 vcc, -1, v63
	v_mfma_f32_32x32x16_bf16 v[32:47], v[132:135], v[204:207], v[32:47]
	s_cbranch_vccnz .Lk_s04
.Lk_c04:
	v_mfma_f32_32x32x16_bf16 v[240:255], v[136:139], v[208:211], v[240:255]
	v_mfma_f32_32x32x16_bf16 v[32:47], v[144:147], v[208:211], v[32:47]
	s_waitcnt vmcnt(4)
	v_max3_u32 v63, v216, v217, v218
	v_max3_u32 v63, v63, v219, v220
	v_mfma_f32_32x32x16_bf16 v[240:255], v[140:143], v[212:215], v[240:255]
	v_max3_u32 v63, v63, v221, v222
	v_max_u32 v63, v63, v223
	v_cmp_eq_u32 vcc, -1, v63
	v_mfma_f32_32x32x16_bf16 v[32:47], v[148:151], v[212:215], v[32:47]
	s_cbranch_vccnz .Lk_s05
.Lk_c05:
	v_mfma_f32_32x32x16_bf16 v[240:255], v[152:155], v[216:219], v[240:255]
	v_mfma_f32_32x32x16_bf16 v[32:47], v[160:163], v[216:219], v[32:47]
	s_waitcnt vmcnt(2)
	v_max3_u32 v63, v224, v225, v226
	v_max3_u32 v63, v63, v227, v228
	v_mfma_f32_32x32x16_bf16 v[240:255], v[156:159], v[220:223], v[240:255]
	v_max3_u32 v63, v63, v229, v230
	v_max_u32 v63, v63, v231
	v_cmp_eq_u32 vcc, -1, v63
	s_cmp_ge_u32 s3, 4
	s_cbranch_scc1 .Lk_cb22
	s_barrier
.Lk_cb22:
	v_mfma_f32_32x32x16_bf16 v[32:47], v[164:167], v[220:223], v[32:47]
	s_cbranch_vccnz .Lk_s06
.Lk_c06:
	v_mfma_f32_32x32x16_bf16 v[240:255], v[168:171], v[224:227], v[240:255]
	v_mfma_f32_32x32x16_bf16 v[32:47], v[176:179], v[224:227], v[32:47]
	v_mfma_f32_32x32x16_bf16 v[240:255], v[172:175], v[228:231], v[240:255]
	v_mfma_f32_32x32x16_bf16 v[32:47], v[180:183], v[228:231], v[32:47]
	s_setprio 0
	s_cmp_lg_u32 s29, 7
	s_cbranch_scc1 .Lk_f23
	buffer_load_dwordx4 v[16:19], v56, s[12:15], s36 offen sc1
	buffer_load_dwordx4 v[20:23], v56, s[12:15], s36 offen offset:1024 sc1
.Lk_f23:
	buffer_load_dwordx4 v[8:11], v56, s[12:15], s37 offen sc1
	buffer_load_dwordx4 v[12:15], v56, s[12:15], s37 offen offset:1024 sc1
	buffer_load_dwordx4 v[24:27], v56, s[12:15], s38 offen sc1
	s_nop 4
	ds_write_b128 v122, v[240:243]
	buffer_load_dwordx4 v[28:31], v56, s[12:15], s38 offen offset:1024 sc1
	ds_write_b128 v122, v[244:247] offset:1056
	buffer_load_dwordx4 v[232:235], v56, s[12:15], s39 offen sc1
	ds_write_b128 v122, v[248:251] offset:2112
	buffer_load_dwordx4 v[236:239], v56, s[12:15], s39 offen offset:1024 sc1
	ds_write_b128 v122, v[252:255] offset:3168
	buffer_load_dwordx4 v[0:3], v56, s[12:15], s55 offen sc1
	ds_write_b128 v122, v[32:35] offset:4224
	buffer_load_dwordx4 v[4:7], v56, s[12:15], s55 offen offset:1024 sc1
	ds_write_b128 v122, v[36:39] offset:5280
	buffer_load_dwordx4 v[184:187], v56, s[12:15], s56 offen sc1
	ds_write_b128 v122, v[40:43] offset:6336
	buffer_load_dwordx4 v[188:191], v56, s[12:15], s56 offen offset:1024 sc1
	ds_write_b128 v122, v[44:47] offset:7392
	buffer_load_dwordx4 v[192:195], v56, s[12:15], s57 offen sc1
	buffer_load_dwordx4 v[196:199], v56, s[12:15], s57 offen offset:1024 sc1
	v_add_u32 v122, 69632, v122
	s_waitcnt lgkmcnt(0)
	s_cmp_ge_u32 s3, 4
	s_cbranch_scc0 .Lk_cb24
	s_barrier
.Lk_cb24:
	s_cmp_lg_u32 s29, 7
	s_cbranch_scc1 .Lk_f25
	s_waitcnt vmcnt(12)
.Lk_c10:
	v_max3_u32 v63, v16, v17, v18
	v_max3_u32 v63, v63, v19, v20
	v_max3_u32 v63, v63, v21, v22
	v_max_u32 v63, v63, v23
	v_cmp_eq_u32 vcc, -1, v63
	s_cbranch_vccnz .Lk_s10
.Lk_f25:
	v_mfma_f32_32x32x16_bf16 v[240:255], v[64:67], v[16:19], 0
	v_mfma_f32_32x32x16_bf16 v[32:47], v[72:75], v[16:19], 0
	s_waitcnt vmcnt(10)
	v_max3_u32 v63, v8, v9, v10
	v_max3_u32 v63, v63, v11, v12
	v_mfma_f32_32x32x16_bf16 v[240:255], v[68:71], v[20:23], v[240:255]
	v_max3_u32 v63, v63, v13, v14
	v_max_u32 v63, v63, v15
	v_cmp_eq_u32 vcc, -1, v63
	v_mfma_f32_32x32x16_bf16 v[32:47], v[76:79], v[20:23], v[32:47]
	s_cbranch_vccnz .Lk_s11
.Lk_c11:
	v_mfma_f32_32x32x16_bf16 v[240:255], v[80:83], v[8:11], v[240:255]
	v_mfma_f32_32x32x16_bf16 v[32:47], v[88:91], v[8:11], v[32:47]
	s_waitcnt vmcnt(8)
	v_max3_u32 v63, v24, v25, v26
	v_max3_u32 v63, v63, v27, v28
	v_mfma_f32_32x32x16_bf16 v[240:255], v[84:87], v[12:15], v[240:255]
	v_max3_u32 v63, v63, v29, v30
	v_max_u32 v63, v63, v31
	v_cmp_eq_u32 vcc, -1, v63
	v_mfma_f32_32x32x16_bf16 v[32:47], v[92:95], v[12:15], v[32:47]
	s_cbranch_vccnz .Lk_s12
.Lk_c12:
	v_mfma_f32_32x32x16_bf16 v[240:255], v[96:99], v[24:27], v[240:255]
	v_mfma_f32_32x32x16_bf16 v[32:47], v[104:107], v[24:27], v[32:47]
	s_waitcnt vmcnt(6)
	v_max3_u32 v63, v232, v233, v234
	v_max3_u32 v63, v63, v235, v236
	v_mfma_f32_32x32x16_bf16 v[240:255], v[100:103], v[28:31], v[240:255]
	v_max3_u32 v63, v63, v237, v238
	v_max_u32 v63, v63, v239
	v_cmp_eq_u32 vcc, -1, v63
	v_mfma_f32_32x32x16_bf16 v[32:47], v[108:111], v[28:31], v[32:47]
	s_cbranch_vccnz .Lk_s13
.Lk_c13:
	v_mfma_f32_32x32x16_bf16 v[240:255], v[112:115], v[232:235], v[240:255]
	v_mfma_f32_32x32x16_bf16 v[32:47], v[128:131], v[232:235], v[32:47]
	s_waitcnt vmcnt(4)
	v_max3_u32 v63, v0, v1, v2
	v_max3_u32 v63, v63, v3, v4
	v_mfma_f32_32x32x16_bf16 v[240:255], v[116:119], v[236:239], v[240:255]
	v_max3_u32 v63, v63, v5, v6
	v_max_u32 v63, v63, v7
	v_cmp_eq_u32 vcc, -1, v63
	v_mfma_f32_32x32x16_bf16 v[32:47], v[132:135], v[236:239], v[32:47]
	s_cbranch_vccnz .Lk_s14
.Lk_c14:
	v_mfma_f32_32x32x16_bf16 v[240:255], v[136:139], v[0:3], v[240:255]
	v_mfma_f32_32x32x16_bf16 v[32:47], v[144:147], v[0:3], v[32:47]
	s_waitcnt vmcnt(2)
	v_max3_u32 v63, v184, v185, v186
	v_max3_u32 v63, v63, v187, v188
	v_mfma_f32_32x32x16_bf16 v[240:255], v[140:143], v[4:7], v[240:255]
	v_max3_u32 v63, v63, v189, v190
	v_max_u32 v63, v63, v191
	v_cmp_eq_u32 vcc, -1, v63
	v_mfma_f32_32x32x16_bf16 v[32:47], v[148:151], v[4:7], v[32:47]
	s_cbranch_vccnz .Lk_s15
.Lk_c15:
	v_mfma_f32_32x32x16_bf16 v[240:255], v[152:155], v[184:187], v[240:255]
	s_waitcnt vmcnt(0)
	v_max3_u32 v63, v192, v193, v194
	v_max3_u32 v63, v63, v195, v196
	v_mfma_f32_32x32x16_bf16 v[240:255], v[156:159], v[188:191], v[240:255]
	v_max3_u32 v63, v63, v197, v198
	v_max_u32 v63, v63, v199
	v_cmp_eq_u32 vcc, -1, v63
	s_cbranch_vccnz .Lk_s16
.Lk_c16:
	v_mfma_f32_32x32x16_bf16 v[240:255], v[168:171], v[192:195], v[240:255]
	s_cmp_ge_u32 s3, 4
	s_cbranch_scc1 .Lk_cb26
	s_barrier
.Lk_cb26:
	v_mfma_f32_32x32x16_bf16 v[240:255], v[172:175], v[196:199], v[240:255]
	v_mfma_f32_32x32x16_bf16 v[32:47], v[160:163], v[184:187], v[32:47]
	v_mfma_f32_32x32x16_bf16 v[32:47], v[164:167], v[188:191], v[32:47]
	s_nop 9
	ds_write_b128 v122, v[240:243]
	v_mfma_f32_32x32x16_bf16 v[32:47], v[176:179], v[192:195], v[32:47]
	ds_write_b128 v122, v[244:247] offset:1056
	v_mfma_f32_32x32x16_bf16 v[32:47], v[180:183], v[196:199], v[32:47]
	ds_write_b128 v122, v[248:251] offset:2112
	s_setprio 0
	ds_write_b128 v122, v[252:255] offset:3168
	s_nop 8
	ds_write_b128 v122, v[32:35] offset:4224
	ds_write_b128 v122, v[36:39] offset:5280
	ds_write_b128 v122, v[40:43] offset:6336
	ds_write_b128 v122, v[44:47] offset:7392
	s_cmp_ge_u32 s3, 4
	s_cbranch_scc1 .Lk_last
	ds_read_b128 v[152:155], v123
	ds_read_b128 v[156:159], v123 offset:8448
	ds_read_b128 v[160:163], v123 offset:16896
	ds_read_b128 v[164:167], v123 offset:25344
	ds_read_b128 v[168:171], v123 offset:33792
	ds_read_b128 v[172:175], v123 offset:42240
	ds_read_b128 v[176:179], v123 offset:50688
	ds_read_b128 v[180:183], v123 offset:59136
	v_lshlrev_b32 v60, 16, v48
	v_and_b32 v61, 0xffff0000, v48
	v_lshlrev_b32 v48, 16, v49
	v_and_b32 v49, 0xffff0000, v49
	s_waitcnt lgkmcnt(6)
	v_add_f32 v152, v152, v156
	v_add_f32 v153, v153, v157
	v_add_f32 v154, v154, v158
	v_add_f32 v155, v155, v159
	v_fmac_f32 v60, v54, v152
	v_fmac_f32 v61, v54, v153
	v_fmac_f32 v48, v54, v154
	v_fmac_f32 v49, v54, v155
	s_waitcnt lgkmcnt(4)
	v_add_f32 v160, v160, v164
	v_add_f32 v161, v161, v165
	v_add_f32 v162, v162, v166
	v_add_f32 v163, v163, v167
	v_fmac_f32 v60, v54, v160
	v_fmac_f32 v61, v54, v161
	v_fmac_f32 v48, v54, v162
	v_fmac_f32 v49, v54, v163
	s_waitcnt lgkmcnt(2)
	v_add_f32 v168, v168, v172
	v_add_f32 v169, v169, v173
	v_add_f32 v170, v170, v174
	v_add_f32 v171, v171, v175
	v_fmac_f32 v60, v54, v168
	v_fmac_f32 v61, v54, v169
	v_fmac_f32 v48, v54, v170
	v_fmac_f32 v49, v54, v171
	s_waitcnt lgkmcnt(0)
	v_add_f32 v176, v176, v180
	v_add_f32 v177, v177, v181
	v_add_f32 v178, v178, v182
	v_add_f32 v179, v179, v183
	v_fmac_f32 v60, v54, v176
	v_fmac_f32 v61, v54, v177
	v_fmac_f32 v48, v54, v178
	v_fmac_f32 v49, v54, v179
	v_cvt_pk_bf16_f32 v60, v60, v61
	v_cvt_pk_bf16_f32 v61, v48, v49
	buffer_store_dwordx2 v[60:61], v124, s[20:23], s42 offen sc1
	ds_read_b128 v[152:155], v123 offset:256
	ds_read_b128 v[156:159], v123 offset:8704
	ds_read_b128 v[160:163], v123 offset:17152
	ds_read_b128 v[164:167], v123 offset:25600
	ds_read_b128 v[168:171], v123 offset:34048
	ds_read_b128 v[172:175], v123 offset:42496
	ds_read_b128 v[176:179], v123 offset:50944
	ds_read_b128 v[180:183], v123 offset:59392
	v_lshlrev_b32 v60, 16, v50
	v_and_b32 v61, 0xffff0000, v50
	v_lshlrev_b32 v50, 16, v51
	v_and_b32 v51, 0xffff0000, v51
	s_waitcnt lgkmcnt(6)
	v_add_f32 v152, v152, v156
	v_add_f32 v153, v153, v157
	v_add_f32 v154, v154, v158
	v_add_f32 v155, v155, v159
	v_fmac_f32 v60, v55, v152
	v_fmac_f32 v61, v55, v153
	v_fmac_f32 v50, v55, v154
	v_fmac_f32 v51, v55, v155
	s_waitcnt lgkmcnt(4)
	v_add_f32 v160, v160, v164
	v_add_f32 v161, v161, v165
	v_add_f32 v162, v162, v166
	v_add_f32 v163, v163, v167
	v_fmac_f32 v60, v55, v160
	v_fmac_f32 v61, v55, v161
	v_fmac_f32 v50, v55, v162
	v_fmac_f32 v51, v55, v163
	s_waitcnt lgkmcnt(2)
	v_add_f32 v168, v168, v172
	v_add_f32 v169, v169, v173
	v_add_f32 v170, v170, v174
	v_add_f32 v171, v171, v175
	v_fmac_f32 v60, v55, v168
	v_fmac_f32 v61, v55, v169
	v_fmac_f32 v50, v55, v170
	v_fmac_f32 v51, v55, v171
	s_waitcnt lgkmcnt(0)
	v_add_f32 v176, v176, v180
	v_add_f32 v177, v177, v181
	v_add_f32 v178, v178, v182
	v_add_f32 v179, v179, v183
	v_fmac_f32 v60, v55, v176
	v_fmac_f32 v61, v55, v177
	v_fmac_f32 v50, v55, v178
	v_fmac_f32 v51, v55, v179
	v_cvt_pk_bf16_f32 v60, v60, v61
	v_cvt_pk_bf16_f32 v61, v50, v51
	buffer_store_dwordx2 v[60:61], v124, s[20:23], s43 offen sc1
.Lk_last:
	v_add_u32 v123, 69632, v123
	s_waitcnt lgkmcnt(0)
	s_barrier
	ds_read_b128 v[152:155], v123
	ds_read_b128 v[156:159], v123 offset:8448
	ds_read_b128 v[160:163], v123 offset:16896
	ds_read_b128 v[164:167], v123 offset:25344
	ds_read_b128 v[168:171], v123 offset:33792
	ds_read_b128 v[172:175], v123 offset:42240
	ds_read_b128 v[176:179], v123 offset:50688
	ds_read_b128 v[180:183], v123 offset:59136
	v_lshlrev_b32 v60, 16, v52
	v_and_b32 v61, 0xffff0000, v52
	v_lshlrev_b32 v52, 16, v53
	v_and_b32 v53, 0xffff0000, v53
	s_waitcnt lgkmcnt(6)
	v_add_f32 v152, v152, v156
	v_add_f32 v153, v153, v157
	v_add_f32 v154, v154, v158
	v_add_f32 v155, v155, v159
	v_fmac_f32 v60, v57, v152
	v_fmac_f32 v61, v57, v153
	v_fmac_f32 v52, v57, v154
	v_fmac_f32 v53, v57, v155
	s_waitcnt lgkmcnt(4)
	v_add_f32 v160, v160, v164
	v_add_f32 v161, v161, v165
	v_add_f32 v162, v162, v166
	v_add_f32 v163, v163, v167
	v_fmac_f32 v60, v57, v160
	v_fmac_f32 v61, v57, v161
	v_fmac_f32 v52, v57, v162
	v_fmac_f32 v53, v57, v163
	s_waitcnt lgkmcnt(2)
	v_add_f32 v168, v168, v172
	v_add_f32 v169, v169, v173
	v_add_f32 v170, v170, v174
	v_add_f32 v171, v171, v175
	v_fmac_f32 v60, v57, v168
	v_fmac_f32 v61, v57, v169
	v_fmac_f32 v52, v57, v170
	v_fmac_f32 v53, v57, v171
	s_waitcnt lgkmcnt(0)
	v_add_f32 v176, v176, v180
	v_add_f32 v177, v177, v181
	v_add_f32 v178, v178, v182
	v_add_f32 v179, v179, v183
	v_fmac_f32 v60, v57, v176
	v_fmac_f32 v61, v57, v177
	v_fmac_f32 v52, v57, v178
	v_fmac_f32 v53, v57, v179
	v_cvt_pk_bf16_f32 v60, v60, v61
	v_cvt_pk_bf16_f32 v61, v52, v53
	buffer_store_dwordx2 v[60:61], v124, s[20:23], s44 offen sc1
	s_branch .Lk_end
.Lk_lowt:
	buffer_load_dwordx4 v[64:67], v121, s[12:15], s41 offen sc1
	buffer_load_dwordx4 v[68:71], v121, s[12:15], s41 offen offset:1024 sc1
	buffer_load_dwordx4 v[72:75], v121, s[12:15], s41 offen offset:2048 sc1
	buffer_load_dwordx4 v[76:79], v121, s[12:15], s41 offen offset:3072 sc1
	s_add_u32 s41, s41, 4096
	buffer_load_dwordx4 v[80:83], v121, s[12:15], s41 offen sc1
	buffer_load_dwordx4 v[84:87], v121, s[12:15], s41 offen offset:1024 sc1
	buffer_load_dwordx4 v[88:91], v121, s[12:15], s41 offen offset:2048 sc1
	buffer_load_dwordx4 v[92:95], v121, s[12:15], s41 offen offset:3072 sc1
	s_add_u32 s41, s41, 4096
	buffer_load_dwordx4 v[96:99], v121, s[12:15], s41 offen sc1
	buffer_load_dwordx4 v[100:103], v121, s[12:15], s41 offen offset:1024 sc1
	buffer_load_dwordx4 v[104:107], v121, s[12:15], s41 offen offset:2048 sc1
	buffer_load_dwordx4 v[108:111], v121, s[12:15], s41 offen offset:3072 sc1
	s_add_u32 s41, s41, 4096
	buffer_load_dwordx4 v[112:115], v121, s[12:15], s41 offen sc1
	buffer_load_dwordx4 v[116:119], v121, s[12:15], s41 offen offset:1024 sc1
	s_branch .Lk_attbar
.Lk_hiC:
	s_add_u32 s41, s41, 2048
	buffer_load_dwordx4 v[0:3], v121, s[12:15], s41 offen sc1
	buffer_load_dwordx4 v[4:7], v121, s[12:15], s41 offen offset:1024 sc1
	buffer_load_dwordx4 v[8:11], v121, s[12:15], s41 offen offset:2048 sc1
	buffer_load_dwordx4 v[12:15], v121, s[12:15], s41 offen offset:3072 sc1
	s_add_u32 s41, s41, 4096
	buffer_load_dwordx4 v[16:19], v121, s[12:15], s41 offen sc1
	buffer_load_dwordx4 v[20:23], v121, s[12:15], s41 offen offset:1024 sc1
	buffer_load_dwordx4 v[24:27], v121, s[12:15], s41 offen offset:2048 sc1
	buffer_load_dwordx4 v[28:31], v121, s[12:15], s41 offen offset:3072 sc1
	s_add_u32 s41, s41, 4096
	buffer_load_dwordx4 v[32:35], v121, s[12:15], s41 offen sc1
	buffer_load_dwordx4 v[36:39], v121, s[12:15], s41 offen offset:1024 sc1
	buffer_load_dwordx4 v[40:43], v121, s[12:15], s41 offen offset:2048 sc1
	s_sub_u32 s59, s41, 114688
	v_add_u32 v57, s43, v121
	buffer_load_dwordx4 v[44:47], v121, s[12:15], s59 offen sc1
	buffer_load_dwordx4 v[52:55], v121, s[12:15], s59 offen offset:1024 sc1
	buffer_load_dwordx4 v[60:63], v121, s[12:15], s59 offen offset:2048 sc1
	v_subrev_u32 v57, 45056, v57
	s_lshl_b32 s59, s3, 13
	s_add_u32 s51, s59, s54
	s_add_u32 s51, s51, 6291456
	s_add_u32 s48, s51, 4096
	v_add_u32 v56, s59, v120
	s_lshl_b32 s59, s3, 11
	s_lshl_b32 s52, s28, 13
	s_add_u32 s52, s52, s59
	s_add_u32 s52, s52, 19914752
	s_add_u32 s59, s59, 147456
	v_add_u32 v59, s59, v121
	s_barrier
	s_waitcnt vmcnt(13)
	ds_write_b128 v57, v[0:3] offset:45056
	s_waitcnt vmcnt(12)
	ds_write_b128 v57, v[4:7] offset:46080
	s_waitcnt vmcnt(11)
	ds_write_b128 v57, v[8:11] offset:47104
	s_waitcnt vmcnt(10)
	ds_write_b128 v57, v[12:15] offset:48128
	s_waitcnt vmcnt(9)
	ds_write_b128 v57, v[16:19] offset:49152
	s_waitcnt vmcnt(8)
	ds_write_b128 v57, v[20:23] offset:50176
	s_waitcnt vmcnt(7)
	ds_write_b128 v57, v[24:27] offset:51200
	s_waitcnt vmcnt(6)
	ds_write_b128 v57, v[28:31] offset:52224
	s_waitcnt vmcnt(5)
	ds_write_b128 v57, v[32:35] offset:53248
	s_waitcnt vmcnt(4)
	ds_write_b128 v57, v[36:39] offset:54272
	s_waitcnt vmcnt(3)
	ds_write_b128 v57, v[40:43] offset:55296
	s_waitcnt vmcnt(2)
	ds_write_b128 v57, v[44:47] offset:8192
	s_waitcnt vmcnt(1)
	ds_write_b128 v57, v[52:55] offset:9216
	s_waitcnt vmcnt(0)
	ds_write_b128 v57, v[60:63] offset:10240
	s_sleep 2
	buffer_load_dwordx4 v[0:3], v121, s[12:15], s50 offen sc1
	buffer_load_dwordx4 v[4:7], v121, s[12:15], s50 offen offset:1024 sc1
	buffer_load_dwordx4 v[8:11], v121, s[12:15], s50 offen offset:2048 sc1
	buffer_load_dwordx4 v[12:15], v121, s[12:15], s50 offen offset:3072 sc1
	buffer_load_dwordx4 v[16:19], v121, s[12:15], s51 offen sc1
	buffer_load_dwordx4 v[20:23], v121, s[12:15], s51 offen offset:1024 sc1
	buffer_load_dwordx4 v[24:27], v121, s[12:15], s51 offen offset:2048 sc1
	buffer_load_dwordx4 v[28:31], v121, s[12:15], s51 offen offset:3072 sc1
	buffer_load_dwordx4 v[32:35], v121, s[12:15], s48 offen sc1
	buffer_load_dwordx4 v[36:39], v121, s[12:15], s48 offen offset:1024 sc1
	buffer_load_dwordx4 v[40:43], v121, s[12:15], s48 offen offset:2048 sc1
	buffer_load_dwordx4 v[44:47], v121, s[12:15], s48 offen offset:3072 sc1
	buffer_load_dwordx4 v[60:63], v121, s[12:15], s52 offen sc1
	buffer_load_dwordx4 v[52:55], v121, s[12:15], s52 offen offset:1024 sc1
.Lk_hld1:
	s_waitcnt vmcnt(10)
	s_cmp_eq_u32 s3, s29
	s_cbranch_scc1 .Lk_hown1
	v_max3_u32 v57, v0, v1, v2
	v_max3_u32 v57, v57, v3, v4
	v_max3_u32 v57, v57, v5, v6
	v_max3_u32 v57, v57, v7, v8
	v_max3_u32 v57, v57, v9, v10
	v_max3_u32 v57, v57, v11, v12
	v_max3_u32 v57, v57, v13, v14
	v_max_u32 v57, v57, v15
	v_cmp_eq_u32 vcc, -1, v57
	s_cbranch_vccnz .Lk_hagain1
	ds_write_b128 v58, v[0:3] offset:24576
	ds_write_b128 v58, v[4:7] offset:25600
	ds_write_b128 v58, v[8:11] offset:26624
	ds_write_b128 v58, v[12:15] offset:27648
.Lk_hown1:
	s_waitcnt lgkmcnt(0)
	s_barrier
	s_waitcnt vmcnt(0)
.Lk_h1chk:
	v_max3_u32 v57, v16, v17, v18
	v_max3_u32 v57, v57, v19, v20
	v_max3_u32 v57, v57, v21, v22
	v_max3_u32 v57, v57, v23, v24
	v_max3_u32 v57, v57, v25, v26
	v_max3_u32 v57, v57, v27, v28
	v_max3_u32 v57, v57, v29, v30
	v_max3_u32 v57, v57, v31, v32
	v_max3_u32 v57, v57, v33, v34
	v_max3_u32 v57, v57, v35, v36
	v_max3_u32 v57, v57, v37, v38
	v_max3_u32 v57, v57, v39, v40
	v_max3_u32 v57, v57, v41, v42
	v_max3_u32 v57, v57, v43, v44
	v_max3_u32 v57, v57, v45, v46
	v_max3_u32 v57, v57, v47, v60
	v_max3_u32 v57, v57, v61, v62
	v_max3_u32 v57, v57, v63, v52
	v_max3_u32 v57, v57, v53, v54
	v_max_u32 v57, v57, v55
	v_cmp_eq_u32 vcc, -1, v57
	s_cbranch_vccnz .Lk_h1again
	ds_write_b128 v59, v[60:63]
	ds_write_b128 v59, v[52:55] offset:1024
	ds_write_b128 v56, v[16:19]
	ds_write_b128 v56, v[20:23] offset:1024
	ds_write_b128 v56, v[24:27] offset:2048
	ds_write_b128 v56, v[28:31] offset:3072
	ds_write_b128 v56, v[32:35] offset:4096
	ds_write_b128 v56, v[36:39] offset:5120
	ds_write_b128 v56, v[40:43] offset:6144
	ds_write_b128 v56, v[44:47] offset:7168
	s_waitcnt lgkmcnt(0)
	s_barrier
	s_branch .Lk_D
.Lk_oagain:
	s_sub_u32 s61, s61, 1
	s_cbranch_scc1 .Lk_end
	s_sleep 2
	buffer_load_dwordx4 v[16:19], v121, s[12:15], s44 offen sc1
	buffer_load_dwordx4 v[20:23], v121, s[12:15], s44 offen offset:1024 sc1
	buffer_load_dwordx4 v[24:27], v121, s[12:15], s44 offen offset:2048 sc1
	buffer_load_dwordx4 v[28:31], v121, s[12:15], s44 offen offset:3072 sc1
	buffer_load_dwordx4 v[32:35], v121, s[12:15], s48 offen sc1
	buffer_load_dwordx4 v[36:39], v121, s[12:15], s48 offen offset:1024 sc1
	buffer_load_dwordx4 v[40:43], v121, s[12:15], s48 offen offset:2048 sc1
	buffer_load_dwordx4 v[44:47], v121, s[12:15], s48 offen offset:3072 sc1
	s_branch .Lk_ochk
.Lk_hagain0:
	s_sub_u32 s61, s61, 1
	s_cbranch_scc1 .Lk_end
	s_waitcnt vmcnt(0)
	s_sleep 2
	buffer_load_dwordx4 v[0:3], v121, s[12:15], s50 offen sc1
	buffer_load_dwordx4 v[4:7], v121, s[12:15], s50 offen offset:1024 sc1
	buffer_load_dwordx4 v[8:11], v121, s[12:15], s50 offen offset:2048 sc1
	buffer_load_dwordx4 v[12:15], v121, s[12:15], s50 offen offset:3072 sc1
	s_branch .Lk_hld0
.Lk_hagain1:
	s_sub_u32 s61, s61, 1
	s_cbranch_scc1 .Lk_end
	s_waitcnt vmcnt(0)
	s_sleep 2
	buffer_load_dwordx4 v[0:3], v121, s[12:15], s50 offen sc1
	buffer_load_dwordx4 v[4:7], v121, s[12:15], s50 offen offset:1024 sc1
	buffer_load_dwordx4 v[8:11], v121, s[12:15], s50 offen offset:2048 sc1
	buffer_load_dwordx4 v[12:15], v121, s[12:15], s50 offen offset:3072 sc1
	buffer_load_dwordx4 v[16:19], v121, s[12:15], s51 offen sc1
	buffer_load_dwordx4 v[20:23], v121, s[12:15], s51 offen offset:1024 sc1
	buffer_load_dwordx4 v[24:27], v121, s[12:15], s51 offen offset:2048 sc1
	buffer_load_dwordx4 v[28:31], v121, s[12:15], s51 offen offset:3072 sc1
	buffer_load_dwordx4 v[32:35], v121, s[12:15], s48 offen sc1
	buffer_load_dwordx4 v[36:39], v121, s[12:15], s48 offen offset:1024 sc1
	buffer_load_dwordx4 v[40:43], v121, s[12:15], s48 offen offset:2048 sc1
	buffer_load_dwordx4 v[44:47], v121, s[12:15], s48 offen offset:3072 sc1
	buffer_load_dwordx4 v[60:63], v121, s[12:15], s52 offen sc1
	buffer_load_dwordx4 v[52:55], v121, s[12:15], s52 offen offset:1024 sc1
	s_branch .Lk_hld1
.Lk_h1again:
	s_sub_u32 s61, s61, 1
	s_cbranch_scc1 .Lk_end
	s_sleep 2
	buffer_load_dwordx4 v[16:19], v121, s[12:15], s51 offen sc1
	buffer_load_dwordx4 v[20:23], v121, s[12:15], s51 offen offset:1024 sc1
	buffer_load_dwordx4 v[24:27], v121, s[12:15], s51 offen offset:2048 sc1
	buffer_load_dwordx4 v[28:31], v121, s[12:15], s51 offen offset:3072 sc1
	buffer_load_dwordx4 v[32:35], v121, s[12:15], s48 offen sc1
	buffer_load_dwordx4 v[36:39], v121, s[12:15], s48 offen offset:1024 sc1
	buffer_load_dwordx4 v[40:43], v121, s[12:15], s48 offen offset:2048 sc1
	buffer_load_dwordx4 v[44:47], v121, s[12:15], s48 offen offset:3072 sc1
	buffer_load_dwordx4 v[60:63], v121, s[12:15], s52 offen sc1
	buffer_load_dwordx4 v[52:55], v121, s[12:15], s52 offen offset:1024 sc1
	s_waitcnt vmcnt(0)
	s_branch .Lk_h1chk
.Lk_s00:
	s_sub_u32 s61, s61, 1
	s_cbranch_scc1 .Lk_end
	s_sleep 2
	buffer_load_dwordx4 v[0:3], v121, s[12:15], s36 offen sc1
	buffer_load_dwordx4 v[4:7], v121, s[12:15], s36 offen offset:1024 sc1
	buffer_load_dwordx4 v[184:187], v121, s[12:15], s37 offen sc1
	buffer_load_dwordx4 v[188:191], v121, s[12:15], s37 offen offset:1024 sc1
	buffer_load_dwordx4 v[192:195], v121, s[12:15], s38 offen sc1
	buffer_load_dwordx4 v[196:199], v121, s[12:15], s38 offen offset:1024 sc1
	buffer_load_dwordx4 v[200:203], v121, s[12:15], s39 offen sc1
	buffer_load_dwordx4 v[204:207], v121, s[12:15], s39 offen offset:1024 sc1
	buffer_load_dwordx4 v[208:211], v121, s[12:15], s55 offen sc1
	buffer_load_dwordx4 v[212:215], v121, s[12:15], s55 offen offset:1024 sc1
	buffer_load_dwordx4 v[216:219], v121, s[12:15], s56 offen sc1
	buffer_load_dwordx4 v[220:223], v121, s[12:15], s56 offen offset:1024 sc1
	buffer_load_dwordx4 v[224:227], v121, s[12:15], s57 offen sc1
	buffer_load_dwordx4 v[228:231], v121, s[12:15], s57 offen offset:1024 sc1
	s_waitcnt vmcnt(0)
	s_branch .Lk_c00
.Lk_s01:
	s_sub_u32 s61, s61, 1
	s_cbranch_scc1 .Lk_end
	s_sleep 2
	buffer_load_dwordx4 v[184:187], v121, s[12:15], s37 offen sc1
	buffer_load_dwordx4 v[188:191], v121, s[12:15], s37 offen offset:1024 sc1
	buffer_load_dwordx4 v[192:195], v121, s[12:15], s38 offen sc1
	buffer_load_dwordx4 v[196:199], v121, s[12:15], s38 offen offset:1024 sc1
	buffer_load_dwordx4 v[200:203], v121, s[12:15], s39 offen sc1
	buffer_load_dwordx4 v[204:207], v121, s[12:15], s39 offen offset:1024 sc1
	buffer_load_dwordx4 v[208:211], v121, s[12:15], s55 offen sc1
	buffer_load_dwordx4 v[212:215], v121, s[12:15], s55 offen offset:1024 sc1
	buffer_load_dwordx4 v[216:219], v121, s[12:15], s56 offen sc1
	buffer_load_dwordx4 v[220:223], v121, s[12:15], s56 offen offset:1024 sc1
	buffer_load_dwordx4 v[224:227], v121, s[12:15], s57 offen sc1
	buffer_load_dwordx4 v[228:231], v121, s[12:15], s57 offen offset:1024 sc1
	s_waitcnt vmcnt(0)
	v_max3_u32 v63, v184, v185, v186
	v_max3_u32 v63, v63, v187, v188
	v_max3_u32 v63, v63, v189, v190
	v_max_u32 v63, v63, v191
	v_cmp_eq_u32 vcc, -1, v63
	s_cbranch_vccnz .Lk_s01
	s_branch .Lk_c01
.Lk_s02:
	s_sub_u32 s61, s61, 1
	s_cbranch_scc1 .Lk_end
	s_sleep 2
	buffer_load_dwordx4 v[192:195], v121, s[12:15], s38 offen sc1
	buffer_load_dwordx4 v[196:199], v121, s[12:15], s38 offen offset:1024 sc1
	buffer_load_dwordx4 v[200:203], v121, s[12:15], s39 offen sc1
	buffer_load_dwordx4 v[204:207], v121, s[12:15], s39 offen offset:1024 sc1
	buffer_load_dwordx4 v[208:211], v121, s[12:15], s55 offen sc1
	buffer_load_dwordx4 v[212:215], v121, s[12:15], s55 offen offset:1024 sc1
	buffer_load_dwordx4 v[216:219], v121, s[12:15], s56 offen sc1
	buffer_load_dwordx4 v[220:223], v121, s[12:15], s56 offen offset:1024 sc1
	buffer_load_dwordx4 v[224:227], v121, s[12:15], s57 offen sc1
	buffer_load_dwordx4 v[228:231], v121, s[12:15], s57 offen offset:1024 sc1
	s_waitcnt vmcnt(0)
	v_max3_u32 v63, v192, v193, v194
	v_max3_u32 v63, v63, v195, v196
	v_max3_u32 v63, v63, v197, v198
	v_max_u32 v63, v63, v199
	v_cmp_eq_u32 vcc, -1, v63
	s_cbranch_vccnz .Lk_s02
	s_branch .Lk_c02
.Lk_s03:
	s_sub_u32 s61, s61, 1
	s_cbranch_scc1 .Lk_end
	s_sleep 2
	buffer_load_dwordx4 v[200:203], v121, s[12:15], s39 offen sc1
	buffer_load_dwordx4 v[204:207], v121, s[12:15], s39 offen offset:1024 sc1
	buffer_load_dwordx4 v[208:211], v121, s[12:15], s55 offen sc1
	buffer_load_dwordx4 v[212:215], v121, s[12:15], s55 offen offset:1024 sc1
	buffer_load_dwordx4 v[216:219], v121, s[12:15], s56 offen sc1
	buffer_load_dwordx4 v[220:223], v121, s[12:15], s56 offen offset:1024 sc1
	buffer_load_dwordx4 v[224:227], v121, s[12:15], s57 offen sc1
	buffer_load_dwordx4 v[228:231], v121, s[12:15], s57 offen offset:1024 sc1
	s_waitcnt vmcnt(0)
	v_max3_u32 v63, v200, v201, v202
	v_max3_u32 v63, v63, v203, v204
	v_max3_u32 v63, v63, v205, v206
	v_max_u32 v63, v63, v207
	v_cmp_eq_u32 vcc, -1, v63
	s_cbranch_vccnz .Lk_s03
	s_branch .Lk_c03
.Lk_s04:
	s_sub_u32 s61, s61, 1
	s_cbranch_scc1 .Lk_end
	s_sleep 2
	buffer_load_dwordx4 v[208:211], v121, s[12:15], s55 offen sc1
	buffer_load_dwordx4 v[212:215], v121, s[12:15], s55 offen offset:1024 sc1
	buffer_load_dwordx4 v[216:219], v121, s[12:15], s56 offen sc1
	buffer_load_dwordx4 v[220:223], v121, s[12:15], s56 offen offset:1024 sc1
	buffer_load_dwordx4 v[224:227], v121, s[12:15], s57 offen sc1
	buffer_load_dwordx4 v[228:231], v121, s[12:15], s57 offen offset:1024 sc1
	s_waitcnt vmcnt(0)
	v_max3_u32 v63, v208, v209, v210
	v_max3_u32 v63, v63, v211, v212
	v_max3_u32 v63, v63, v213, v214
	v_max_u32 v63, v63, v215
	v_cmp_eq_u32 vcc, -1, v63
	s_cbranch_vccnz .Lk_s04
	s_branch .Lk_c04
.Lk_s05:
	s_sub_u32 s61, s61, 1
	s_cbranch_scc1 .Lk_end
	s_sleep 2
	buffer_load_dwordx4 v[216:219], v121, s[12:15], s56 offen sc1
	buffer_load_dwordx4 v[220:223], v121, s[12:15], s56 offen offset:1024 sc1
	buffer_load_dwordx4 v[224:227], v121, s[12:15], s57 offen sc1
	buffer_load_dwordx4 v[228:231], v121, s[12:15], s57 offen offset:1024 sc1
	s_waitcnt vmcnt(0)
	v_max3_u32 v63, v216, v217, v218
	v_max3_u32 v63, v63, v219, v220
	v_max3_u32 v63, v63, v221, v222
	v_max_u32 v63, v63, v223
	v_cmp_eq_u32 vcc, -1, v63
	s_cbranch_vccnz .Lk_s05
	s_branch .Lk_c05
.Lk_s06:
	s_sub_u32 s61, s61, 1
	s_cbranch_scc1 .Lk_end
	s_sleep 2
	buffer_load_dwordx4 v[224:227], v121, s[12:15], s57 offen sc1
	buffer_load_dwordx4 v[228:231], v121, s[12:15], s57 offen offset:1024 sc1
	s_waitcnt vmcnt(0)
	v_max3_u32 v63, v224, v225, v226
	v_max3_u32 v63, v63, v227, v228
	v_max3_u32 v63, v63, v229, v230
	v_max_u32 v63, v63, v231
	v_cmp_eq_u32 vcc, -1, v63
	s_cbranch_vccnz .Lk_s06
	s_branch .Lk_c06
.Lk_s10:
	s_sub_u32 s61, s61, 1
	s_cbranch_scc1 .Lk_end
	s_sleep 2
	buffer_load_dwordx4 v[16:19], v56, s[12:15], s36 offen sc1
	buffer_load_dwordx4 v[20:23], v56, s[12:15], s36 offen offset:1024 sc1
	buffer_load_dwordx4 v[8:11], v56, s[12:15], s37 offen sc1
	buffer_load_dwordx4 v[12:15], v56, s[12:15], s37 offen offset:1024 sc1
	buffer_load_dwordx4 v[24:27], v56, s[12:15], s38 offen sc1
	buffer_load_dwordx4 v[28:31], v56, s[12:15], s38 offen offset:1024 sc1
	buffer_load_dwordx4 v[232:235], v56, s[12:15], s39 offen sc1
	buffer_load_dwordx4 v[236:239], v56, s[12:15], s39 offen offset:1024 sc1
	buffer_load_dwordx4 v[0:3], v56, s[12:15], s55 offen sc1
	buffer_load_dwordx4 v[4:7], v56, s[12:15], s55 offen offset:1024 sc1
	buffer_load_dwordx4 v[184:187], v56, s[12:15], s56 offen sc1
	buffer_load_dwordx4 v[188:191], v56, s[12:15], s56 offen offset:1024 sc1
	buffer_load_dwordx4 v[192:195], v56, s[12:15], s57 offen sc1
	buffer_load_dwordx4 v[196:199], v56, s[12:15], s57 offen offset:1024 sc1
	s_waitcnt vmcnt(0)
	s_branch .Lk_c10
.Lk_s11:
	s_sub_u32 s61, s61, 1
	s_cbranch_scc1 .Lk_end
	s_sleep 2
	buffer_load_dwordx4 v[8:11], v56, s[12:15], s37 offen sc1
	buffer_load_dwordx4 v[12:15], v56, s[12:15], s37 offen offset:1024 sc1
	buffer_load_dwordx4 v[24:27], v56, s[12:15], s38 offen sc1
	buffer_load_dwordx4 v[28:31], v56, s[12:15], s38 offen offset:1024 sc1
	buffer_load_dwordx4 v[232:235], v56, s[12:15], s39 offen sc1
	buffer_load_dwordx4 v[236:239], v56, s[12:15], s39 offen offset:1024 sc1
	buffer_load_dwordx4 v[0:3], v56, s[12:15], s55 offen sc1
	buffer_load_dwordx4 v[4:7], v56, s[12:15], s55 offen offset:1024 sc1
	buffer_load_dwordx4 v[184:187], v56, s[12:15], s56 offen sc1
	buffer_load_dwordx4 v[188:191], v56, s[12:15], s56 offen offset:1024 sc1
	buffer_load_dwordx4 v[192:195], v56, s[12:15], s57 offen sc1
	buffer_load_dwordx4 v[196:199], v56, s[12:15], s57 offen offset:1024 sc1
	s_waitcnt vmcnt(0)
	v_max3_u32 v63, v8, v9, v10
	v_max3_u32 v63, v63, v11, v12
	v_max3_u32 v63, v63, v13, v14
	v_max_u32 v63, v63, v15
	v_cmp_eq_u32 vcc, -1, v63
	s_cbranch_vccnz .Lk_s11
	s_branch .Lk_c11
.Lk_s12:
	s_sub_u32 s61, s61, 1
	s_cbranch_scc1 .Lk_end
	s_sleep 2
	buffer_load_dwordx4 v[24:27], v56, s[12:15], s38 offen sc1
	buffer_load_dwordx4 v[28:31], v56, s[12:15], s38 offen offset:1024 sc1
	buffer_load_dwordx4 v[232:235], v56, s[12:15], s39 offen sc1
	buffer_load_dwordx4 v[236:239], v56, s[12:15], s39 offen offset:1024 sc1
	buffer_load_dwordx4 v[0:3], v56, s[12:15], s55 offen sc1
	buffer_load_dwordx4 v[4:7], v56, s[12:15], s55 offen offset:1024 sc1
	buffer_load_dwordx4 v[184:187], v56, s[12:15], s56 offen sc1
	buffer_load_dwordx4 v[188:191], v56, s[12:15], s56 offen offset:1024 sc1
	buffer_load_dwordx4 v[192:195], v56, s[12:15], s57 offen sc1
	buffer_load_dwordx4 v[196:199], v56, s[12:15], s57 offen offset:1024 sc1
	s_waitcnt vmcnt(0)
	v_max3_u32 v63, v24, v25, v26
	v_max3_u32 v63, v63, v27, v28
	v_max3_u32 v63, v63, v29, v30
	v_max_u32 v63, v63, v31
	v_cmp_eq_u32 vcc, -1, v63
	s_cbranch_vccnz .Lk_s12
	s_branch .Lk_c12
.Lk_s13:
	s_sub_u32 s61, s61, 1
	s_cbranch_scc1 .Lk_end
	s_sleep 2
	buffer_load_dwordx4 v[232:235], v56, s[12:15], s39 offen sc1
	buffer_load_dwordx4 v[236:239], v56, s[12:15], s39 offen offset:1024 sc1
	buffer_load_dwordx4 v[0:3], v56, s[12:15], s55 offen sc1
	buffer_load_dwordx4 v[4:7], v56, s[12:15], s55 offen offset:1024 sc1
	buffer_load_dwordx4 v[184:187], v56, s[12:15], s56 offen sc1
	buffer_load_dwordx4 v[188:191], v56, s[12:15], s56 offen offset:1024 sc1
	buffer_load_dwordx4 v[192:195], v56, s[12:15], s57 offen sc1
	buffer_load_dwordx4 v[196:199], v56, s[12:15], s57 offen offset:1024 sc1
	s_waitcnt vmcnt(0)
	v_max3_u32 v63, v232, v233, v234
	v_max3_u32 v63, v63, v235, v236
	v_max3_u32 v63, v63, v237, v238
	v_max_u32 v63, v63, v239
	v_cmp_eq_u32 vcc, -1, v63
	s_cbranch_vccnz .Lk_s13
	s_branch .Lk_c13
.Lk_s14:
	s_sub_u32 s61, s61, 1
	s_cbranch_scc1 .Lk_end
	s_sleep 2
	buffer_load_dwordx4 v[0:3], v56, s[12:15], s55 offen sc1
	buffer_load_dwordx4 v[4:7], v56, s[12:15], s55 offen offset:1024 sc1
	buffer_load_dwordx4 v[184:187], v56, s[12:15], s56 offen sc1
	buffer_load_dwordx4 v[188:191], v56, s[12:15], s56 offen offset:1024 sc1
	buffer_load_dwordx4 v[192:195], v56, s[12:15], s57 offen sc1
	buffer_load_dwordx4 v[196:199], v56, s[12:15], s57 offen offset:1024 sc1
	s_waitcnt vmcnt(0)
	v_max3_u32 v63, v0, v1, v2
	v_max3_u32 v63, v63, v3, v4
	v_max3_u32 v63, v63, v5, v6
	v_max_u32 v63, v63, v7
	v_cmp_eq_u32 vcc, -1, v63
	s_cbranch_vccnz .Lk_s14
	s_branch .Lk_c14
.Lk_s15:
	s_sub_u32 s61, s61, 1
	s_cbranch_scc1 .Lk_end
	s_sleep 2
	buffer_load_dwordx4 v[184:187], v56, s[12:15], s56 offen sc1
	buffer_load_dwordx4 v[188:191], v56, s[12:15], s56 offen offset:1024 sc1
	buffer_load_dwordx4 v[192:195], v56, s[12:15], s57 offen sc1
	buffer_load_dwordx4 v[196:199], v56, s[12:15], s57 offen offset:1024 sc1
	s_waitcnt vmcnt(0)
	v_max3_u32 v63, v184, v185, v186
	v_max3_u32 v63, v63, v187, v188
	v_max3_u32 v63, v63, v189, v190
	v_max_u32 v63, v63, v191
	v_cmp_eq_u32 vcc, -1, v63
	s_cbranch_vccnz .Lk_s15
	s_branch .Lk_c15
.Lk_s16:
	s_sub_u32 s61, s61, 1
	s_cbranch_scc1 .Lk_end
	s_sleep 2
	buffer_load_dwordx4 v[192:195], v56, s[12:15], s57 offen sc1
	buffer_load_dwordx4 v[196:199], v56, s[12:15], s57 offen offset:1024 sc1
	s_waitcnt vmcnt(0)
	v_max3_u32 v63, v192, v193, v194
	v_max3_u32 v63, v63, v195, v196
	v_max3_u32 v63, v63, v197, v198
	v_max_u32 v63, v63, v199
	v_cmp_eq_u32 vcc, -1, v63
	s_cbranch_vccnz .Lk_s16
	s_branch .Lk_c16
.Lk_re2:
	s_sub_u32 s61, s61, 1
	s_cbranch_scc1 .Lk_end
	s_sleep 2
	buffer_load_dword v144, v148, s[12:15], s27 offen sc1
	s_waitcnt vmcnt(0)
	s_branch .Lk_ev2
.Lk_la:
	v_lshlrev_b32 v151, 7, v126
	s_mov_b32 s59, 20185088
	buffer_load_dword v156, v151, s[12:15], s59 offen sc1
	s_mov_b32 s59, 20193280
	buffer_load_dword v157, v151, s[12:15], s59 offen sc1
	s_mov_b32 s59, 20201472
	buffer_load_dword v158, v151, s[12:15], s59 offen sc1
	s_mov_b32 s59, 20209664
	buffer_load_dword v159, v151, s[12:15], s59 offen sc1
	s_branch .Lk_lar
.Lk_beat:
	v_min3_u32 v156, v156, v157, v158
	v_min_u32 v156, v156, v159
	v_cmp_eq_u32 vcc, 0, v156
	s_cbranch_vccz .Lk_ev2
	s_sub_u32 s61, s61, 1
	s_cbranch_scc1 .Lk_end
	s_add_u32 s60, s60, 1
	v_mov_b32 v152, s60
	buffer_store_dword v152, off, s[12:15], s58 sc1
	s_sleep 2
	v_lshlrev_b32 v151, 7, v126
	s_mov_b32 s59, 20185088
	buffer_load_dword v156, v151, s[12:15], s59 offen sc1
	s_mov_b32 s59, 20193280
	buffer_load_dword v157, v151, s[12:15], s59 offen sc1
	s_mov_b32 s59, 20201472
	buffer_load_dword v158, v151, s[12:15], s59 offen sc1
	s_mov_b32 s59, 20209664
	buffer_load_dword v159, v151, s[12:15], s59 offen sc1
	s_waitcnt vmcnt(0)
	s_branch .Lk_beat
.Lk_zero:
	buffer_load_dword v119, off, s[12:15], s58 sc1
	v_mov_b32 v112, 0
	v_mov_b32 v113, 0
	v_mov_b32 v114, 0
	v_mov_b32 v115, 0
	v_bfe_u32 v116, v0, 1, 5
	v_and_b32 v117, 1, v0
	v_lshlrev_b32 v116, 7, v116
	v_lshl_or_b32 v116, v117, 4, v116
	v_lshl_or_b32 v116, s52, 12, v116
	s_mov_b32 s59, 20185088
	buffer_store_dwordx4 v[112:115], v116, s[12:15], s59 offen sc1
	s_mov_b32 s59, 20217856
	buffer_store_dwordx4 v[112:115], v116, s[12:15], s59 offen sc1
	s_waitcnt vmcnt(7)
	s_branch .Lk_zret
.Lk_slow:
	s_waitcnt vmcnt(0)
	buffer_load_dwordx2 v[144:145], v149, s[12:15], s26 offen sc1
	s_cmp_lg_u32 s62, 0
	s_cbranch_scc1 .Lk_f27
	buffer_store_dwordx4 v[252:255], v121, s[12:15], s33 offen sc1
.Lk_f27:
	s_waitcnt vmcnt(0)
.Lk_e29:
	v_min_u32 v144, v144, v145
	v_cmp_eq_u32 vcc, 0, v144
	s_cbranch_vccnz .Lk_e29r
	buffer_load_dwordx4 v[128:131], v121, s[12:15], s39 offen sc1
	s_cmp_lg_u32 s62, 1
	s_cbranch_scc1 .Lk_f28
	buffer_store_dwordx4 v[252:255], v121, s[12:15], s33 offen sc1
.Lk_f28:
	buffer_load_dwordx4 v[132:135], v121, s[12:15], s39 offen offset:1024 sc1
	s_cmp_lg_u32 s62, 0
	s_cbranch_scc1 .Lk_f30
	buffer_store_dwordx4 v[252:255], v121, s[12:15], s35 offen sc1
.Lk_f30:
	s_memrealtime s[26:27]
	buffer_load_dwordx4 v[136:139], v121, s[12:15], s39 offen offset:2048 sc1
	buffer_load_dwordx4 v[140:143], v121, s[12:15], s39 offen offset:3072 sc1
	s_add_u32 s39, s39, 4096
	s_cmp_lg_u32 s62, 1
	s_cbranch_scc1 .Lk_f31
	buffer_store_dwordx4 v[252:255], v121, s[12:15], s35 offen sc1
.Lk_f31:
	buffer_load_dwordx4 v[144:147], v121, s[12:15], s39 offen sc1
	buffer_load_dwordx4 v[148:151], v121, s[12:15], s39 offen offset:1024 sc1
	buffer_load_dwordx4 v[152:155], v121, s[12:15], s39 offen offset:2048 sc1
	buffer_load_dwordx4 v[156:159], v121, s[12:15], s39 offen offset:3072 sc1
	s_add_u32 s39, s39, 4096
	buffer_load_dwordx4 v[160:163], v121, s[12:15], s39 offen sc1
	buffer_store_dwordx4 v[252:255], v121, s[12:15], s24 offen sc1
	s_sub_u32 s24, s24, 0x8000
	buffer_load_dwordx4 v[164:167], v121, s[12:15], s39 offen offset:1024 sc1
	buffer_store_dwordx4 v[252:255], v121, s[12:15], s24 offen sc1
	buffer_load_dwordx4 v[168:171], v121, s[12:15], s39 offen offset:2048 sc1
	buffer_load_dwordx4 v[172:175], v121, s[12:15], s39 offen offset:3072 sc1
	s_add_u32 s39, s39, 4096
	buffer_load_dwordx4 v[176:179], v121, s[12:15], s39 offen sc1
	s_cmp_lg_u32 s62, 0
	s_cbranch_scc1 .Lk_f32
	buffer_store_dwordx4 v[252:255], v121, s[12:15], s35 offen offset:1024 sc1
.Lk_f32:
	buffer_load_dwordx4 v[180:183], v121, s[12:15], s39 offen offset:1024 sc1
	s_cmp_lg_u32 s62, 1
	s_cbranch_scc1 .Lk_f33
	buffer_store_dwordx4 v[252:255], v121, s[12:15], s35 offen offset:1024 sc1
.Lk_f33:
	s_memrealtime s[24:25]
	buffer_load_dwordx4 v[184:187], v121, s[12:15], s39 offen offset:2048 sc1
	buffer_load_dwordx4 v[188:191], v121, s[12:15], s39 offen offset:3072 sc1
	s_add_u32 s39, s39, 4096
	s_waitcnt vmcnt(6)
	s_waitcnt lgkmcnt(0)
	s_sub_u32 s26, s24, s26
	s_lshr_b32 s26, s26, 1
	s_add_u32 s24, s24, s26
	s_or_b32 s24, s24, 1
	v_mov_b32 v248, s24
	s_mov_b64 exec, 1
	buffer_store_dword v248, off, s[12:15], s48 sc1
	s_mov_b64 exec, -1
	buffer_load_dwordx4 v[192:195], v121, s[12:15], s39 offen sc1
	buffer_load_dwordx4 v[196:199], v121, s[12:15], s39 offen offset:1024 sc1
	buffer_load_dwordx4 v[200:203], v121, s[12:15], s39 offen offset:2048 sc1
	buffer_load_dwordx4 v[204:207], v121, s[12:15], s39 offen offset:3072 sc1
	s_add_u32 s39, s39, 4096
	buffer_load_dwordx4 v[208:211], v121, s[12:15], s39 offen sc1
	buffer_load_dwordx4 v[212:215], v121, s[12:15], s39 offen offset:1024 sc1
	s_cmp_lg_u32 s62, 0
	s_cbranch_scc1 .Lk_f34
	buffer_store_dwordx4 v[252:255], v121, s[12:15], s45 offen sc1
.Lk_f34:
	buffer_load_dwordx4 v[216:219], v121, s[12:15], s39 offen offset:2048 sc1
	buffer_load_dwordx4 v[220:223], v121, s[12:15], s39 offen offset:3072 sc1
	s_add_u32 s39, s39, 4096
	s_cmp_lg_u32 s62, 1
	s_cbranch_scc1 .Lk_f35
	buffer_store_dwordx4 v[252:255], v121, s[12:15], s45 offen sc1
.Lk_f35:
	buffer_load_dwordx4 v[224:227], v121, s[12:15], s39 offen sc1
	s_cmp_lg_u32 s62, 0
	s_cbranch_scc1 .Lk_f36
	buffer_store_dwordx4 v[252:255], v121, s[12:15], s45 offen offset:1024 sc1
.Lk_f36:
	buffer_load_dwordx4 v[228:231], v121, s[12:15], s39 offen offset:1024 sc1
	s_cmp_lg_u32 s62, 1
	s_cbranch_scc1 .Lk_f37
	buffer_store_dwordx4 v[252:255], v121, s[12:15], s45 offen offset:1024 sc1
.Lk_f37:
	buffer_load_dwordx4 v[232:235], v121, s[12:15], s39 offen offset:2048 sc1
	buffer_load_dwordx4 v[236:239], v121, s[12:15], s39 offen offset:3072 sc1
	s_add_u32 s39, s39, 4096
	buffer_load_dwordx4 v[240:243], v121, s[12:15], s39 offen sc1
	buffer_load_dwordx4 v[244:247], v121, s[12:15], s39 offen offset:1024 sc1
	buffer_load_dword v125, v124, s[12:15], s51 offen sc1
	buffer_load_dwordx4 v[248:251], v121, s[12:15], s39 offen offset:2048 sc1
	buffer_load_dwordx4 v[252:255], v121, s[12:15], s39 offen offset:3072 sc1
	v_mov_b32 v0, 0
	v_mov_b32 v1, 0
	v_mov_b32 v2, 0
	v_mov_b32 v3, 0
	v_mov_b32 v4, 0
	v_mov_b32 v5, 0
	v_mov_b32 v6, 0
	v_mov_b32 v7, 0
	v_mov_b32 v8, 0
	v_mov_b32 v9, 0
	v_mov_b32 v10, 0
	v_mov_b32 v11, 0
	v_mov_b32 v12, 0
	v_mov_b32 v13, 0
	v_mov_b32 v14, 0
	v_mov_b32 v15, 0
	v_mov_b32 v16, 0
	v_mov_b32 v17, 0
	v_mov_b32 v18, 0
	v_mov_b32 v19, 0
	v_mov_b32 v20, 0
	v_mov_b32 v21, 0
	v_mov_b32 v22, 0
	v_mov_b32 v23, 0
	v_mov_b32 v24, 0
	v_mov_b32 v25, 0
	v_mov_b32 v26, 0
	v_mov_b32 v27, 0
	v_mov_b32 v28, 0
	v_mov_b32 v29, 0
	v_mov_b32 v30, 0
	v_mov_b32 v31, 0
	s_lshl_b32 s44, s36, 17
	s_mov_b32 s59, 64
	v_mov_b32 v84, 0xff800000
	v_mov_b32 v100, 0xff800000
.Lk_mx:
	buffer_load_dwordx4 v[72:75], v121, s[12:15], s44 offen sc1
	s_waitcnt vmcnt(0)
	v_mfma_f32_32x32x16_bf16 v[32:47], v[72:75], v[64:67], 0
	v_mfma_f32_32x32x16_bf16 v[48:63], v[72:75], v[68:71], 0
	s_add_u32 s44, s44, 2048
	s_nop 9
	v_max3_f32 v84, v84, v32, v33
	v_max3_f32 v84, v84, v34, v35
	v_max3_f32 v84, v84, v36, v37
	v_max3_f32 v84, v84, v38, v39
	v_max3_f32 v84, v84, v40, v41
	v_max3_f32 v84, v84, v42, v43
	v_max3_f32 v84, v84, v44, v45
	v_max3_f32 v84, v84, v46, v47
	v_max3_f32 v100, v100, v48, v49
	v_max3_f32 v100, v100, v50, v51
	v_max3_f32 v100, v100, v52, v53
	v_max3_f32 v100, v100, v54, v55
	v_max3_f32 v100, v100, v56, v57
	v_max3_f32 v100, v100, v58, v59
	v_max3_f32 v100, v100, v60, v61
	v_max3_f32 v100, v100, v62, v63
	s_sub_u32 s59, s59, 1
	s_cmp_lg_u32 s59, 0
	s_cbranch_scc1 .Lk_mx
	v_mov_b32 v85, v84
	s_nop 1
	v_permlane32_swap_b32 v84, v85
	v_max_f32 v84, v84, v85
	v_sub_f32 v84, 0, v84
	v_mov_b32 v85, v84
	v_mov_b32 v86, v84
	v_mov_b32 v87, v84
	v_mov_b32 v88, v84
	v_mov_b32 v89, v84
	v_mov_b32 v90, v84
	v_mov_b32 v91, v84
	v_mov_b32 v92, v84
	v_mov_b32 v93, v84
	v_mov_b32 v94, v84
	v_mov_b32 v95, v84
	v_mov_b32 v96, v84
	v_mov_b32 v97, v84
	v_mov_b32 v98, v84
	v_mov_b32 v99, v84
	v_mov_b32 v101, v100
	s_nop 1
	v_permlane32_swap_b32 v100, v101
	v_max_f32 v100, v100, v101
	v_sub_f32 v100, 0, v100
	v_mov_b32 v101, v100
	v_mov_b32 v102, v100
	v_mov_b32 v103, v100
	v_mov_b32 v104, v100
	v_mov_b32 v105, v100
	v_mov_b32 v106, v100
	v_mov_b32 v107, v100
	v_mov_b32 v108, v100
	v_mov_b32 v109, v100
	v_mov_b32 v110, v100
	v_mov_b32 v111, v100
	v_mov_b32 v112, v100
	v_mov_b32 v113, v100
	v_mov_b32 v114, v100
	v_mov_b32 v115, v100
	s_mov_b32 s44, s38
	s_mov_b32 s59, 32
.Lk_sl:
	buffer_load_dwordx4 v[72:75], v127, s[12:15], s44 offen sc1
	s_mov_b64 exec, s[46:47]
	buffer_load_dwordx4 v[76:79], v122, s[12:15], s44 offen offset:1024 sc1
	buffer_load_dwordx4 v[80:83], v122, s[12:15], s44 offen offset:1536 sc1
	s_mov_b64 exec, -1
	s_waitcnt vmcnt(0)
	v_mfma_f32_32x32x16_bf16 v[32:47], v[72:75], v[64:67], v[84:99]
	v_mfma_f32_32x32x16_bf16 v[48:63], v[72:75], v[68:71], v[100:115]
	s_add_u32 s44, s44, 4096
	s_nop 9
	v_exp_f32 v32, v32
	v_exp_f32 v33, v33
	v_exp_f32 v34, v34
	v_exp_f32 v35, v35
	v_exp_f32 v36, v36
	v_exp_f32 v37, v37
	v_exp_f32 v38, v38
	v_exp_f32 v39, v39
	v_exp_f32 v40, v40
	v_exp_f32 v41, v41
	v_exp_f32 v42, v42
	v_exp_f32 v43, v43
	v_exp_f32 v44, v44
	v_exp_f32 v45, v45
	v_exp_f32 v46, v46
	v_exp_f32 v47, v47
	v_exp_f32 v48, v48
	v_exp_f32 v49, v49
	v_exp_f32 v50, v50
	v_exp_f32 v51, v51
	v_exp_f32 v52, v52
	v_exp_f32 v53, v53
	v_exp_f32 v54, v54
	v_exp_f32 v55, v55
	v_exp_f32 v56, v56
	v_exp_f32 v57, v57
	v_exp_f32 v58, v58
	v_exp_f32 v59, v59
	v_exp_f32 v60, v60
	v_exp_f32 v61, v61
	v_exp_f32 v62, v62
	v_exp_f32 v63, v63
	v_smfmac_f32_32x32x32_bf16 v[0:15], v[76:79], v[32:39], v120
	v_smfmac_f32_32x32x32_bf16 v[0:15], v[80:83], v[40:47], v120
	v_smfmac_f32_32x32x32_bf16 v[16:31], v[76:79], v[48:55], v120
	v_smfmac_f32_32x32x32_bf16 v[16:31], v[80:83], v[56:63], v120
	s_sub_u32 s59, s59, 1
	s_cmp_lg_u32 s59, 0
	s_cbranch_scc1 .Lk_sl
	s_branch .Lk_attdone
.Lk_e6r:
	s_sub_u32 s61, s61, 1
	s_cbranch_scc1 .Lk_end
	s_sleep 2
	buffer_load_dwordx2 v[144:145], v149, s[12:15], s26 offen sc1
	s_waitcnt vmcnt(0)
	s_branch .Lk_e6
.Lk_e29r:
	s_sub_u32 s61, s61, 1
	s_cbranch_scc1 .Lk_end
	s_sleep 2
	buffer_load_dwordx2 v[144:145], v149, s[12:15], s26 offen sc1
	s_waitcnt vmcnt(0)
	s_branch .Lk_e29
.Lk_end:
)ASM"
      : "+{v0}"(tid)
      : "{s[0:1]}"(__builtin_amdgcn_kernarg_segment_ptr()), "{s2}"(blockIdx.x)
      : "v1", "v2", "v3", "v4", "v5", "v6", "v7", "v8", "v9", "v10", "v11", "v12", "v13", "v14", "v15", "v16", "v17", "v18", "v19", "v20", "v21", "v22", "v23", "v24", "v25", "v26", "v27", "v28", "v29", "v30", "v31", "v32", "v33", "v34", "v35", "v36", "v37", "v38", "v39", "v40", "v41", "v42", "v43", "v44", "v45", "v46", "v47", "v48", "v49", "v50", "v51", "v52", "v53", "v54", "v55", "v56", "v57", "v58", "v59", "v60", "v61", "v62", "v63", "v64", "v65", "v66", "v67", "v68", "v69", "v70", "v71", "v72", "v73", "v74", "v75", "v76", "v77", "v78", "v79", "v80", "v81", "v82", "v83", "v84", "v85", "v86", "v87", "v88", "v89", "v90", "v91", "v92", "v93", "v94", "v95", "v96", "v97", "v98", "v99", "v100", "v101", "v102", "v103", "v104", "v105", "v106", "v107", "v108", "v109", "v110", "v111", "v112", "v113", "v114", "v115", "v116", "v117", "v118", "v119", "v120", "v121", "v122", "v123", "v124", "v125", "v126", "v127", "v128", "v129", "v130", "v131", "v132", "v133", "v134", "v135", "v136", "v137", "v138", "v139", "v140", "v141", "v142", "v143", "v144", "v145", "v146", "v147", "v148", "v149", "v150", "v151", "v152", "v153", "v154", "v155", "v156", "v157", "v158", "v159", "v160", "v161", "v162", "v163", "v164", "v165", "v166", "v167", "v168", "v169", "v170", "v171", "v172", "v173", "v174", "v175", "v176", "v177", "v178", "v179", "v180", "v181", "v182", "v183", "v184", "v185", "v186", "v187", "v188", "v189", "v190", "v191", "v192", "v193", "v194", "v195", "v196", "v197", "v198", "v199", "v200", "v201", "v202", "v203", "v204", "v205", "v206", "v207", "v208", "v209", "v210", "v211", "v212", "v213", "v214", "v215", "v216", "v217", "v218", "v219", "v220", "v221", "v222", "v223", "v224", "v225", "v226", "v227", "v228", "v229", "v230", "v231", "v232", "v233", "v234", "v235", "v236", "v237", "v238", "v239", "v240", "v241", "v242", "v243", "v244", "v245", "v246", "v247", "v248", "v249", "v250", "v251", "v252", "v253", "v254", "v255", "s3", "s4", "s5", "s6", "s7", "s8", "s9", "s10", "s11", "s12", "s13", "s14", "s15", "s16", "s17", "s18", "s19", "s20", "s21", "s22", "s23", "s24", "s25", "s26", "s27", "s28", "s29", "s30", "s31", "s33", "s34", "s35", "s36", "s37", "s38", "s39", "s40", "s41", "s42", "s43", "s44", "s45", "s46", "s47", "s48", "s49", "s50", "s51", "s52", "s53", "s54", "s55", "s56", "s57", "s58", "s59", "s60", "s61", "s62", "s63", "vcc", "scc", "memory");
}

extern "C" void kernel_launch(void* const* d_in, const int* in_sizes, int n_in, void* d_out, int out_size, void* d_ws, size_t ws_size,
                              hipStream_t stream) {
  (void)hipFuncSetAttribute((const void*)k_block, hipFuncAttributeMaxDynamicSharedMemorySize, 163840);
  k_block<<<256, 512, 163840, stream>>>(d_in[0], d_in[1], d_in[2], d_in[4], d_in[5], d_out, d_ws);
}
```
